```python
import torch

def _softmax(x, axis=-1):
    return torch.softmax(x, dim=axis)
def _gelu(x):
    return torch.nn.functional.gelu(x, approximate="tanh")
def _silu(x):
    return torch.nn.functional.silu(x)
def _sigmoid(x):
    return torch.sigmoid(x)
def _layernorm(x, axis=-1, eps=1e-5):
    m = x.mean(dim=axis, keepdim=True)
    v = x.var(dim=axis, keepdim=True, unbiased=False)
    return (x - m) / torch.sqrt(v + eps)
def _scatter_add(tgt, idx, src, axis=0):
    return tgt.clone().index_add_(axis, idx, src)
def _segment_sum(src, seg, n):
    out = torch.zeros((n,) + src.shape[1:], dtype=src.dtype)
    return out.index_add_(0, seg, src)
def _pad(x, pw):
    flat = tuple(v for pair in reversed(pw) for v in pair)
    return torch.nn.functional.pad(x, flat)

kernel_name = 'concat_heads__ssm_conv_h3__grouped_kv_mqa__prompt'

def setup_inputs():

    _w = (lambda a: (lambda t: t.to(torch.bfloat16) if t.is_floating_point() else t)(torch.as_tensor(a)))
    def _A_setup():
        B, S, D = 1, 2048, 640
        ks = 4
        return {
            "q": _w((lambda *s: torch.randn(*s, dtype=torch.bfloat16) * 0.05)(B, S, D)),
            "k": _w((lambda *s: torch.randn(*s, dtype=torch.bfloat16) * 0.4)(B, S, D)),
            "v": _w((lambda *s: torch.randn(*s, dtype=torch.bfloat16) * 0.2)(B, S, D)),
            "shift_k": _w((lambda *s: torch.randn(*s, dtype=torch.bfloat16) * 0.4)(D, ks) * 0.5),
            "ssm_k": _w((lambda *s: torch.randn(*s, dtype=torch.bfloat16) * 0.4)(D, S) * (1.0 / S ** 0.5)),
            "ssm_D": _w((lambda *s: torch.randn(*s, dtype=torch.bfloat16) * 0.2)(D)),
        }
    _A_in = _A_setup()

    def _B_setup():
        B, S, D, H = 1, 2048, 640, 32
        Dh = D // H
        return {
            "x": _w((lambda *s: torch.randn(*s, dtype=torch.bfloat16) * 0.05)(B, S, D)),
            "wq": _w((lambda *s: torch.randn(*s, dtype=torch.bfloat16) * 0.8)(D, H * Dh)),
            "wk": _w((lambda *s: torch.randn(*s, dtype=torch.bfloat16) * 0.8)(D, Dh)),
            "wv": _w((lambda *s: torch.randn(*s, dtype=torch.bfloat16) * 0.05)(D, Dh)),
            "wo": _w((lambda *s: torch.randn(*s, dtype=torch.bfloat16) * 0.05)(H * Dh, D)),
        }
    _B_in = _B_setup()

    return {
        "A_q": _A_in["q"],
        "A_k": _A_in["k"],
        "A_v": _A_in["v"],
        "A_shift_k": _A_in["shift_k"],
        "A_ssm_k": _A_in["ssm_k"],
        "A_ssm_D": _A_in["ssm_D"],
        "B_x": _B_in["x"],
        "B_wq": _B_in["wq"],
        "B_wk": _B_in["wk"],
        "B_wv": _B_in["wv"],
        "B_wo": _B_in["wo"],
    }

def reference(A_q, A_k, A_v, A_shift_k, A_ssm_k, A_ssm_D, B_x, B_wq, B_wk, B_wv, B_wo):
    def _A_ref(q, k, v, shift_k, ssm_k, ssm_D):
        B, S, D = q.shape
        ks = shift_k.shape[-1]
        k_pad = _pad(k, ((0, 0), (ks - 1, 0), (0, 0)))
        k_shift = torch.zeros_like(k)
        for t in range(ks):
            k_shift = k_shift + k_pad[:, t:t + S, :] * shift_k[:, ks - 1 - t][None, None, :]
        kv = k_shift * v
        L = 2 * S
        kv_pad = _pad(kv, ((0, 0), (0, S), (0, 0)))
        kf_pad = _pad(ssm_k, ((0, 0), (0, S)))
        kv_f = (lambda x, axes=(-2,-1): torch.fft.rfft2(x.float(), dim=axes))(kv_pad[:, None, :, :], axes=(1, 2))
        kf_f = (lambda x, axes=(-2,-1): torch.fft.rfft2(x.float(), dim=axes))(kf_pad[:, None, :], axes=(1, 2))
        kf_f = (lambda x, *d: x.permute(*d))(kf_f, 1, 2, 0)
        y_f = kv_f * kf_f[None, :, :, :]
        y = (lambda x, s, axes=(-2,-1): torch.fft.irfft2(x, s=s, dim=axes).to(torch.bfloat16))(y_f, (1, L), axes=(1, 2))[:, 0, :S, :]
        y = y + kv * ssm_D[None, None, :]
        return q * y

    def _B_ref(x, wq, wk, wv, wo):
        B, S, D = x.shape
        H = 32
        Dh = D // H
        q = (lambda x, *d: x.permute(*d))((x @ wq).reshape(B, S, H, Dh), 0, 2, 1, 3)
        k = x @ wk
        v = x @ wv
        scores = torch.einsum("bhqd,bkd->bhqk", q, k) * (Dh ** -0.5)
        p = _softmax(scores, axis=-1)
        o = torch.einsum("bhqk,bkd->bhqd", p, v)
        return (lambda x, *d: x.permute(*d))(o, 0, 2, 1, 3).reshape(B, S, H * Dh) @ wo

    a = _A_ref(A_q, A_k, A_v, A_shift_k, A_ssm_k, A_ssm_D)
    b = _B_ref(B_x, B_wq, B_wk, B_wv, B_wo)
    return torch.concatenate([a, b], axis=-1)
```

```cpp
#include <hip/hip_runtime.h>
#include <hip/hip_bf16.h>
typedef unsigned short u16;
__device__ __forceinline__ float b2f(u16 x) { return __uint_as_float(((unsigned)x) << 16); }
__device__ __forceinline__ u16 f2b(float f) { __bf16 b = (__bf16)f; return __builtin_bit_cast(u16, b); }
constexpr int S = 2048, D = 640, H = 32, DH = 20;

__global__ void k_gemm(const u16* A, const u16* B, u16* C, int N, int ldc) {
  int idx = blockIdx.x * blockDim.x + threadIdx.x;
  if (idx >= S * N) return;
  int r = idx / N, c = idx % N;
  float acc = 0;
  for (int k = 0; k < D; k++) acc += b2f(A[r * D + k]) * b2f(B[k * N + c]);
  C[r * ldc + c] = f2b(acc);
}
__global__ void k_attn(const u16* Q, const u16* K, const u16* V, u16* O) {
  int idx = blockIdx.x * blockDim.x + threadIdx.x;
  int r = idx / H, h = idx % H;
  float q[DH], o[DH];
  for (int d = 0; d < DH; d++) { q[d] = b2f(Q[r * D + h * DH + d]) * 0.22360679775f; o[d] = 0; }
  float m = -1e30f, l = 0;
  for (int j = 0; j < S; j++) {
    float s = 0;
    for (int d = 0; d < DH; d++) s += q[d] * b2f(K[j * DH + d]);
    float mn = fmaxf(m, s);
    float a = __expf(m - mn), p = __expf(s - mn);
    l = l * a + p;
    for (int d = 0; d < DH; d++) o[d] = o[d] * a + p * b2f(V[j * DH + d]);
    m = mn;
  }
  for (int d = 0; d < DH; d++) O[r * D + h * DH + d] = f2b(o[d] / l);
}
__global__ void k_kv(const u16* k, const u16* v, const u16* sh, u16* kv) {
  int idx = blockIdx.x * blockDim.x + threadIdx.x;
  int s = idx / D, d = idx % D;
  float acc = 0;
  for (int j = 3; j >= 0; j--) if (s - j >= 0) acc += b2f(k[(s - j) * D + d]) * b2f(sh[d * 4 + j]);
  kv[idx] = f2b(acc * b2f(v[idx]));
}
__global__ void k_conv(const u16* q, const u16* kv, const u16* h, const u16* Dv, u16* out) {
  int idx = blockIdx.x * blockDim.x + threadIdx.x;
  int s = idx / D, d = idx % D;
  float acc = 0;
  for (int u = 0; u <= s; u++) acc += b2f(kv[u * D + d]) * b2f(h[d * S + s - u]);
  float y = acc + b2f(kv[idx]) * b2f(Dv[d]);
  out[s * 2 * D + d] = f2b(b2f(q[idx]) * y);
}
extern "C" void kernel_launch(void* const* d_in, const int* in_sizes, int n_in,
                              void* d_out, int out_size, void* d_ws, size_t ws_size,
                              hipStream_t stream) {
  const u16* Aq = (const u16*)d_in[0]; const u16* Ak = (const u16*)d_in[1]; const u16* Av = (const u16*)d_in[2];
  const u16* sh = (const u16*)d_in[3]; const u16* hk = (const u16*)d_in[4]; const u16* Dv = (const u16*)d_in[5];
  const u16* x = (const u16*)d_in[6]; const u16* wq = (const u16*)d_in[7]; const u16* wk = (const u16*)d_in[8];
  const u16* wv = (const u16*)d_in[9]; const u16* wo = (const u16*)d_in[10];
  u16* out = (u16*)d_out;
  u16* ws = (u16*)d_ws;
  u16* Q = ws; u16* K = Q + S * D; u16* V = K + S * DH; u16* O = V + S * DH; u16* kv = O + S * D;
  k_gemm<<<S * D / 256, 256, 0, stream>>>(x, wq, Q, D, D);
  k_gemm<<<S * DH / 256, 256, 0, stream>>>(x, wk, K, DH, DH);
  k_gemm<<<S * DH / 256, 256, 0, stream>>>(x, wv, V, DH, DH);
  k_attn<<<S * H / 256, 256, 0, stream>>>(Q, K, V, O);
  k_gemm<<<S * D / 256, 256, 0, stream>>>(O, wo, out + D, D, 2 * D);
  k_kv<<<S * D / 256, 256, 0, stream>>>(Ak, Av, sh, kv);
  k_conv<<<S * D / 256, 256, 0, stream>>>(Aq, kv, hk, Dv, out);
}
```

```cpp
#include <hip/hip_runtime.h>
#include <hip/hip_bf16.h>

typedef unsigned short u16;
typedef unsigned long long u64;
typedef __attribute__((ext_vector_type(16))) float f32x16;
typedef __attribute__((ext_vector_type(8))) float f32x8;
typedef __attribute__((ext_vector_type(4))) float f32x4;
typedef __attribute__((ext_vector_type(16))) __bf16 bf16x16;
typedef __attribute__((ext_vector_type(8))) __bf16 bf16x8;
typedef __attribute__((ext_vector_type(2))) __bf16 bf16x2;
typedef __attribute__((ext_vector_type(4))) short s16x4;
typedef __attribute__((ext_vector_type(4))) unsigned u32x4;
typedef __attribute__((ext_vector_type(2))) unsigned u32x2;
typedef __amdgpu_buffer_rsrc_t rsrc_t;
#define LAS __attribute__((address_space(3)))
#define GAS __attribute__((address_space(1)))
#define DI __device__ __forceinline__
#define SB() __builtin_amdgcn_sched_barrier(0)

constexpr int S = 2048, D = 640;
constexpr float KSCALE = 0.22360679775f * 1.44269504089f;

constexpr unsigned WS_EPOCH = 0;
constexpr unsigned WS_F1 = 4096;
constexpr unsigned WS_F2 = 8192;
constexpr unsigned WS_F3 = 12288;
constexpr unsigned WS_F4 = 16384;
constexpr unsigned WS_KVP = 1u << 20;
constexpr unsigned WS_IMG = 4u << 20;
constexpr unsigned WS_ZT = 5u << 20;
constexpr unsigned WS_OX = 8u << 20;
constexpr unsigned WS_BYTES = 12u << 20;
constexpr unsigned REC = 5376, K2OFF = 2048, VOFF = 2688, VFR = 672;

constexpr unsigned SLOT = 41984, L_WQ = 16384, L_WKV = 36864;
constexpr unsigned L_KT = 125952, L_VT = L_KT + 11264, L_SH = L_KT + 21504;
constexpr unsigned L_XR = L_KT, XRC = 4608, XRP = 1152;
constexpr unsigned L_HB = 148480, HBC = 4128;
constexpr unsigned L_QX = 2 * SLOT, L_VB = L_QX + 16384;
constexpr unsigned L_WO = 0, L_OP = 102400, L_ZB = 151552, L_RED = L_OP;
constexpr unsigned L_MISC = 163328, L_SPX = L_MISC + 64;
constexpr unsigned LDS_BYTES = 163840;
enum { C_MAIN = 0, C_AUX = 1, C_F1 = 2, C_F2 = 3, C_F3 = 4, C_KMAX = 5, C_QDONE = 6, C_CONV = 7, C_BAD = 8 };

constexpr int SC1 = 16;
constexpr unsigned OOB = 0x80000000u;
constexpr unsigned SPIN_LDS = 1u << 22, SPIN_GLB = 1u << 17;

struct Params { const u16 *Aq, *Ak, *Av, *sh, *hk, *Dv, *x, *wq, *wk, *wv, *wo; u16* out; char* ws; };

DI float lo2f(unsigned v) { return __uint_as_float(v << 16); }
DI float hi2f(unsigned v) { return __uint_as_float(v & 0xffff0000u); }
DI unsigned pk2(float a, float b) { bf16x2 t = {(__bf16)a, (__bf16)b}; return __builtin_bit_cast(unsigned, t); }
DI float dot2(unsigned a, unsigned b, float c) { return __builtin_amdgcn_fdot2_f32_bf16(__builtin_bit_cast(bf16x2, a), __builtin_bit_cast(bf16x2, b), c, false); }
DI rsrc_t mkrsrc(const void* p, unsigned n) { return __builtin_amdgcn_make_buffer_rsrc((void*)p, 0, n, 0x00020000); }
DI void dma16(rsrc_t rs, unsigned voff, unsigned soff, unsigned lds) {
  asm volatile("s_mov_b32 m0, %0\n\ts_nop 0\n\tbuffer_load_dwordx4 %1, %2, %3 offen lds" :: "s"(lds), "v"(voff), "s"(rs), "s"(soff) : "memory");
}
#define WAITV(n) asm volatile("s_waitcnt vmcnt(" #n ")" ::: "memory")
#define WAITL() asm volatile("s_waitcnt lgkmcnt(0)" ::: "memory")
template <typename T> DI LAS T* lp(unsigned a) { return (LAS T*)(size_t)a; }
DI u32x2 trd(unsigned a) { return __builtin_bit_cast(u32x2, __builtin_amdgcn_ds_read_tr16_b64_v4i16(lp<s16x4>(a))); }
DI bf16x8 cat2(u32x2 a, u32x2 b) { return __builtin_bit_cast(bf16x8, __builtin_shufflevector(a, b, 0, 1, 2, 3)); }
DI bf16x8 asb(u32x4 v) { return __builtin_bit_cast(bf16x8, v); }
DI unsigned rfl(unsigned v) { return __builtin_amdgcn_readfirstlane(v); }

DI unsigned cnt_arrive(int c, int lane) {
  unsigned r; u64 save;
  asm volatile("s_waitcnt lgkmcnt(0)\n\ts_mov_b64 %1, exec\n\ts_mov_b64 exec, 1\n\tds_add_rtn_u32 %0, %2, %3\n\ts_mov_b64 exec, %1\n\ts_waitcnt lgkmcnt(0)"
               : "=&v"(r), "=&s"(save) : "v"(L_MISC + 4 * c), "v"(1u) : "memory");
  return rfl(r);
}
DI void cnt_wait(int c, unsigned target) {
#pragma nounroll
  for (unsigned spins = 0; spins < SPIN_LDS; ++spins) {
    unsigned v = __hip_atomic_load(lp<unsigned>(L_MISC + 4 * c), __ATOMIC_RELAXED, __HIP_MEMORY_SCOPE_WORKGROUP);
    if (rfl(v) >= target) break;
  }
  asm volatile("" ::: "memory");
}
DI void grp_bar(int c, unsigned target, int lane) { if (cnt_arrive(c, lane) + 1 < target) cnt_wait(c, target); asm volatile("" ::: "memory"); }

DI void flag_store(char* ws, unsigned off, u64 v) { __hip_atomic_store((GAS u64*)(ws + off), v, __ATOMIC_RELAXED, __HIP_MEMORY_SCOPE_AGENT); }
DI u64 flag_poll(char* ws, unsigned off, bool active, u64 tag) {
  u64 v = 0;
#pragma nounroll
  for (unsigned spins = 0; spins < SPIN_GLB; ++spins) {
    v = __hip_atomic_load((GAS u64*)(ws + off), __ATOMIC_RELAXED, __HIP_MEMORY_SCOPE_AGENT);
    if (__all(!active || (v >> 16) == tag)) break;
    __builtin_amdgcn_s_sleep(1);
  }
  __builtin_amdgcn_fence(__ATOMIC_ACQUIRE, "wavefront");
  asm volatile("" ::: "memory");
  return v;
}

#define MF32(a, b, c) __builtin_amdgcn_mfma_f32_32x32x16_bf16(a, b, c, 0, 0, 0)
#define MF8(a, b, c) __builtin_amdgcn_mfma_f32_32x32x8bf16_1k(a, b, c, 0, 0, 0)
#define MF16(a, b, c) __builtin_amdgcn_mfma_f32_16x16x32_bf16(a, b, c, 0, 0, 0)
#define PV(v, p, o) __builtin_amdgcn_smfmac_f32_32x32x32_bf16(v, p, o, idx, 0, 0)

#define STAMP(i)
#define STAMP_DECL
#define STAMP_OUT()

template <int O> DI bf16x16 sub8(f32x16 v) {
  f32x8 t = __builtin_shufflevector(v, v, O, O + 1, O + 2, O + 3, O + 4, O + 5, O + 6, O + 7);
  return __builtin_bit_cast(bf16x16, t);
}
template <int A, int B> DI void exps(f32x16& s) {
#pragma unroll
  for (int i = A; i < B; i++) s[i] = __builtin_amdgcn_exp2f(s[i]);
}
template <int HALF> DI s16x4 k2h(u32x4 v) { return __builtin_bit_cast(s16x4, __builtin_shufflevector(v, v, 2 * HALF, 2 * HALF + 1)); }
struct Rec { u32x4 k1[2]; u32x4 k2; u32x4 v[4]; };
constexpr int vmenc(int n) { return (n & 15) | ((n >> 4) << 14) | (7 << 4) | (15 << 8); }

template <typename F4, typename F6>
DI void tile(f32x16& sc, f32x16& sprev, f32x16& oprev, f32x16& ocur, bf16x8 vprev_b, bf16x8 vcur_a,
             bf16x8 kn1, s16x4 kn2, bf16x8 qn1, s16x4 qn2, f32x16& sn, int idx, F4 f4, F6 f6) {
  const f32x16 zero = {0};
  SB(); f32x16 t = MF8(kn2, qn2, zero);
  SB(); exps<0, 4>(sc);
  SB(); t = MF32(kn1, qn1, t);
  SB(); exps<4, 8>(sc);
  SB(); oprev = PV(vprev_b, sub8<8>(sprev), oprev);
  SB(); f4();
  SB(); exps<8, 12>(sc);
  SB(); ocur = PV(vcur_a, sub8<0>(sc), ocur);
  SB(); f6();
  SB(); exps<12, 16>(sc);
  SB(); sn = t;
}
DI void loadrec(Rec& r, rsrc_t rs, unsigned vk, unsigned vk2, unsigned vv, unsigned soff) {
  r.k2 = __builtin_amdgcn_raw_buffer_load_b128(rs, vk2, soff, SC1);
  r.k1[0] = __builtin_amdgcn_raw_buffer_load_b128(rs, vk, soff, SC1);
  r.v[0] = __builtin_amdgcn_raw_buffer_load_b128(rs, vv, soff, SC1);
  r.v[1] = __builtin_amdgcn_raw_buffer_load_b128(rs, vv + VFR, soff, SC1);
  r.k1[1] = __builtin_amdgcn_raw_buffer_load_b128(rs, vk + 1024, soff, SC1);
  r.v[2] = __builtin_amdgcn_raw_buffer_load_b128(rs, vv + 2 * VFR, soff, SC1);
  r.v[3] = __builtin_amdgcn_raw_buffer_load_b128(rs, vv + 3 * VFR, soff, SC1);
}
DI void dstep(Rec& c, Rec& n, f32x16 (&s)[3], f32x16 (&o)[4], const bf16x8 (&q1)[4], const s16x4 (&q2)[4], int idx,
              rsrc_t rs, unsigned vk, unsigned vk2, unsigned vv, unsigned soff) {
  auto N = []() {};
#define L_(dst, off) [&]() { dst = __builtin_amdgcn_raw_buffer_load_b128(rs, off, soff, SC1); }
#define W_(n) []() { __builtin_amdgcn_s_waitcnt(vmenc(n)); }
  tile(s[0], s[2], o[3], o[0], asb(n.v[3]), asb(c.v[0]), asb(c.k1[0]), k2h<0>(c.k2), q1[1], q2[1], s[1], idx, L_(n.k2, vk2), L_(n.k1[0], vk));
  tile(s[1], s[0], o[0], o[1], asb(c.v[1]), asb(c.v[0]), asb(c.k1[0]), k2h<0>(c.k2), q1[2], q2[2], s[2], idx, L_(n.v[0], vv), L_(n.v[1], vv + VFR));
  tile(s[2], s[1], o[1], o[2], asb(c.v[1]), asb(c.v[0]), asb(c.k1[0]), k2h<0>(c.k2), q1[3], q2[3], s[0], idx, L_(n.k1[1], vk + 1024), L_(n.v[2], vv + 2 * VFR));
  tile(s[0], s[2], o[2], o[3], asb(c.v[1]), asb(c.v[0]), asb(c.k1[1]), k2h<1>(c.k2), q1[0], q2[0], s[1], idx, L_(n.v[3], vv + 3 * VFR), N);
  tile(s[1], s[0], o[3], o[0], asb(c.v[1]), asb(c.v[2]), asb(c.k1[1]), k2h<1>(c.k2), q1[1], q2[1], s[2], idx, N, N);
  tile(s[2], s[1], o[0], o[1], asb(c.v[3]), asb(c.v[2]), asb(c.k1[1]), k2h<1>(c.k2), q1[2], q2[2], s[0], idx, N, N);
  tile(s[0], s[2], o[1], o[2], asb(c.v[3]), asb(c.v[2]), asb(c.k1[1]), k2h<1>(c.k2), q1[3], q2[3], s[1], idx, N, W_(5));
  tile(s[1], s[0], o[2], o[3], asb(c.v[3]), asb(c.v[2]), asb(n.k1[0]), k2h<0>(n.k2), q1[0], q2[0], s[2], idx, N, W_(0));
  f32x16 a = s[2], bb = s[1]; s[0] = a; s[2] = bb;
#undef L_
#undef W_
}

template <int J>
DI void conv_unit(unsigned hb, unsigned xb, float (&own)[2][4], float (&spl)[2][3]) {
  constexpr int TB = 7 - J, TS = J, NA = 2 * TB + 2;
  bf16x8 a[NA];
#pragma unroll
  for (int n = 0; n < NA; n++) a[n] = cat2(trd(hb + 256 * n), trd(hb + 256 * n + 32));
  f32x4 ab[4], as[4];
#pragma unroll
  for (int r = 0; r < 4; r++) {
    ab[r] = f32x4{0, 0, 0, 0}; as[r] = f32x4{0, 0, 0, 0};
#pragma unroll
    for (int v = -1; v <= 2 * TB; v++) {
      const unsigned ad = xb + r * XRP + 64 * (14 - v);
      u32x2 x0 = *(volatile LAS u32x2*)(size_t)ad, x1 = *(volatile LAS u32x2*)(size_t)(ad + 8);
      bf16x8 bx = cat2(x0, x1);
      ab[r] = MF16(a[2 * TB - v], bx, ab[r]);
      if (v <= 2 * TS) as[r] = MF16(a[2 * TS - v], bx, as[r]);
    }
  }
  own[0][0] = ab[0][0]; own[0][1] = ab[0][1] + ab[1][0]; own[0][2] = ab[0][2] + ab[1][1] + ab[2][0]; own[0][3] = ab[0][3] + ab[1][2] + ab[2][1] + ab[3][0];
  spl[0][0] = ab[1][3] + ab[2][2] + ab[3][1]; spl[0][1] = ab[2][3] + ab[3][2]; spl[0][2] = ab[3][3];
  own[1][0] = as[0][0]; own[1][1] = as[0][1] + as[1][0]; own[1][2] = as[0][2] + as[1][1] + as[2][0]; own[1][3] = as[0][3] + as[1][2] + as[2][1] + as[3][0];
  spl[1][0] = as[1][3] + as[2][2] + as[3][1]; spl[1][1] = as[2][3] + as[3][2]; spl[1][2] = as[3][3];
}

__global__ __launch_bounds__(512, 2) void fused_kernel(Params P) {
  extern __shared__ __attribute__((aligned(16))) char smem_raw[];
  const int tid = threadIdx.x, lane = tid & 63;
  const int wave = rfl(tid >> 6);
  const int b = blockIdx.x;
  const int rt = 4 * (b & 7) + (b >> 6), hg = (b >> 3) & 7;
  const bool isK = hg == (rt & 1), isV = hg == (rt & 1) + 2, isKV = isK || isV;
  const int nch = 2 + ((rt + hg) & 1);
  const int ch0 = 80 * hg + 5 * (rt >> 1) + ((rt & 1) ? 2 + (hg & 1) : 0);
  const int col = lane & 31, h = lane >> 5, g = lane >> 4, c16 = lane & 15, tq = (lane & 15) >> 2, tp = lane & 3;
  const rsrc_t rws = mkrsrc(P.ws, WS_BYTES);
  asm volatile("" :: "v"(smem_raw));
  STAMP_DECL;
  STAMP(0);
  u64 epoch, tag;
#define EPOCH_LOAD() asm volatile("s_load_dwordx2 %0, %1, 0x0" : "=s"(epoch) : "s"(P.ws) : "memory")
#define EPOCH_WAIT() asm volatile("s_waitcnt lgkmcnt(0)" : "+s"(epoch) :: "memory"); epoch += 1; tag = epoch & 0xffffffffffffull
  if (tid < 16) *lp<unsigned>(L_MISC + 4 * tid) = 0;

  bf16x8 q1[4]; s16x4 q2[4];

  if (wave < 4) {
    const int rs = wave & 1, kh = wave >> 1;
    const rsrc_t rx = mkrsrc(P.x, S * D * 2), rwq = mkrsrc(P.wq, D * D * 2), rwkv = mkrsrc(isK ? P.wk : P.wv, D * 40);
    unsigned vx[2], vw[5];
#pragma unroll
    for (int i = 0; i < 2; i++) { int r7 = lane >> 3, pos = lane & 7; vx[i] = (64 * rt + 16 * wave + 8 * i + r7) * 1280 + 16 * (pos ^ (4 * i + (r7 >> 1))); }
#pragma unroll
    for (int i = 0; i < 5; i++) { int u = i * 64 + lane, pr = u / 10, ch = u - pr * 10, kk = (pr & ~7) + ((pr & 1) << 2) + ((pr >> 1) & 3); vw[i] = (32 * wave + kk) * 1280 + 160 * hg + 16 * ch; }
    const unsigned vkv = 1280 * wave + 16 * lane;
    auto issue = [&](int c, unsigned slot) {
      dma16(rx, vx[0], 256 * c, slot + 4096 * wave);
      dma16(rx, vx[0], 256 * c + 128, slot + 4096 * wave + 1024);
      dma16(rx, vx[1], 256 * c, slot + 4096 * wave + 2048);
      dma16(rx, vx[1], 256 * c + 128, slot + 4096 * wave + 3072);
#pragma unroll
      for (int i = 0; i < 5; i++) dma16(rwq, vw[i], 163840 * c, slot + L_WQ + 5120 * wave + 1024 * i);
      if (isKV) {
        dma16(rwkv, vkv, 5120 * c, slot + L_WKV + 1280 * wave);
        if (lane < 16) dma16(rwkv, vkv, 5120 * c + 1024, slot + L_WKV + 1280 * wave + 1024);
      }
    };
    STAMP(7);
    issue(0, 0);
    STAMP(8);
    EPOCH_LOAD();
    issue(1, SLOT); issue(2, 2 * SLOT);
    EPOCH_WAIT();
    STAMP(14);
    __builtin_amdgcn_s_barrier();
    unsigned xa[4];
#pragma unroll
    for (int i = 0; i < 4; i++) xa[i] = (4 * rs + (col >> 3)) * 2048 + (col & 7) * 128 + 16 * ((2 * i + h) ^ ((col >> 1) & 7));
    unsigned wa[2], wst;
    {
      const int blk = g & 1;
      const bool kvl = isKV && kh == 0 && blk == 1 && tp != 0;
      const int pc = blk ? 4 : ((tp & 1) * 2 + (tp >> 1));
#pragma unroll
      for (int t = 0; t < 2; t++) {
        const int kp = tp == 1 ? 4 : (tp - 2) * 2 + t;
        wa[t] = kvl ? L_WKV + (8 * h + tq) * 40 + kp * 8 : L_WQ + (8 * h + 2 * tq) * 160 + (2 * kh + t) * 40 + pc * 8;
      }
      wst = kvl ? 640 : 2560;
    }
    f32x16 acc0 = {0}, acc1 = {0};
    auto compute = [&](unsigned slot) {
#pragma unroll
      for (int ks = 0; ks < 8; ks++) {
        bf16x8 xf = *lp<bf16x8>(slot + xa[ks & 3] + (ks >> 2) * 1024);
        const unsigned w0 = slot + wa[0] + ks * wst, w1 = slot + wa[1] + ks * wst;
        bf16x8 a0 = cat2(trd(w0), trd(w0 + 160)), a1 = cat2(trd(w1), trd(w1 + 160));
        acc0 = MF32(a0, xf, acc0);
        acc1 = MF32(a1, xf, acc1);
      }
    };
    if (isKV) WAITV(22); else WAITV(18);
    STAMP(15);
    grp_bar(C_MAIN, 4, lane);
    STAMP(1);
    compute(0);
    if (isKV) WAITV(11); else WAITV(9);
    grp_bar(C_MAIN, 8, lane);
    issue(3, 0);
    compute(SLOT);
    if (isKV) WAITV(11); else WAITV(9);
    grp_bar(C_MAIN, 12, lane);
    issue(4, SLOT);
    compute(2 * SLOT);
    if (isKV) WAITV(11); else WAITV(9);
    grp_bar(C_MAIN, 16, lane);
    compute(0);
    WAITV(0);
    grp_bar(C_MAIN, 20, lane);
    compute(SLOT);
    STAMP(2);
    if (isKV && kh == 0) {
      const unsigned rec = WS_IMG + rt * REC;
      const float sc = isK ? KSCALE : 1.0f;
      u32x4 w = {pk2(acc0[12] * sc, acc0[13] * sc), pk2(acc0[14] * sc, acc0[15] * sc), pk2(acc1[12] * sc, acc1[13] * sc), pk2(acc1[14] * sc, acc1[15] * sc)};
      u32x2 w2 = {pk2(acc0[8] * sc, acc0[9] * sc), pk2(acc0[10] * sc, acc0[11] * sc)};
      if (isK) {
        __builtin_amdgcn_raw_buffer_store_b128(w, rws, rec + rs * 1024 + lane * 16, 0, SC1);
        u32x2 cst = {0x3f803f80u, 0x00003f80u};
        __builtin_amdgcn_raw_buffer_store_b64(h ? w2 : cst, rws, h ? rec + K2OFF + col * 16 + rs * 8 : (lane == 0 ? rec + K2OFF + 512 + rs * 8 : OOB), 0, SC1);
        float n2 = 0;
#pragma unroll
        for (int i = 0; i < 4; i++) n2 += lo2f(w[i]) * lo2f(w[i]) + hi2f(w[i]) * hi2f(w[i]);
        if (h) n2 += lo2f(w2[0]) * lo2f(w2[0]) + hi2f(w2[0]) * hi2f(w2[0]) + lo2f(w2[1]) * lo2f(w2[1]) + hi2f(w2[1]) * hi2f(w2[1]);
        n2 += __shfl_xor(n2, 32);
        __hip_atomic_fetch_max(lp<unsigned>(L_MISC + 4 * C_KMAX), __float_as_uint(n2), __ATOMIC_RELAXED, __HIP_MEMORY_SCOPE_WORKGROUP);
      } else {
        const unsigned vb = L_VB + rs * 2048;
        *lp<u32x4>(vb + col * 48 + 16 * h) = w;
        if (h) *lp<u32x2>(vb + col * 48 + 32) = w2;
        WAITL();
        const unsigned ra = vb + (8 * g + tq) * 48;
        const unsigned pa = ((tp & 1) * 2 + (tp >> 1)) * 8;
        u32x2 t0 = trd(ra + pa), t1 = trd(ra + pa + 192), t2 = trd(ra + 32), t3 = trd(ra + 32 + 192);
        const unsigned fo = rec + VOFF + (2 * rs + (g >> 1)) * VFR;
        __builtin_amdgcn_raw_buffer_store_b128(u32x4{t0[0], t0[1], t1[0], t1[1]}, rws, fo + ((g & 1) * 20 + c16) * 16, 0, SC1);
        const unsigned cv = c16 == 4 ? 0x3f803f80u : 0u;
        u32x4 e = c16 < 4 ? u32x4{t2[0], t2[1], t3[0], t3[1]} : u32x4{cv, cv, cv, cv};
        const unsigned eo = c16 < 4 ? fo + ((g & 1) * 20 + 16 + c16) * 16 : ((c16 < 6 && !(g & 1)) ? fo + (36 + c16) * 16 : OOB);
        __builtin_amdgcn_raw_buffer_store_b128(e, rws, eo, 0, SC1);
      }
      WAITV(0);
      if (cnt_arrive(C_F2, lane) == 1) {
        unsigned kb = 0;
        if (isK) {
          float km = __builtin_amdgcn_sqrtf(__uint_as_float(__hip_atomic_load(lp<unsigned>(L_MISC + 4 * C_KMAX), __ATOMIC_RELAXED, __HIP_MEMORY_SCOPE_WORKGROUP)));
          kb = (__float_as_uint(km) + 0xffffu) >> 16;
        }
        if (lane == 0) flag_store(P.ws, WS_F2 + ((isK ? 0 : 32) + rt) * 8, (tag << 16) | kb);
      }
    }
    u32x2 qx2[2];
#pragma unroll
    for (int t = 0; t < 2; t++) {
      const f32x16& a = t ? acc1 : acc0;
      u32x4 w = {pk2(a[0], a[1]), pk2(a[2], a[3]), pk2(a[4], a[5]), pk2(a[6], a[7])};
      u32x2 w2 = {pk2(a[8], a[9]), pk2(a[10], a[11])};
      float n2 = 0;
#pragma unroll
      for (int i = 0; i < 4; i++) n2 += lo2f(w[i]) * lo2f(w[i]) + hi2f(w[i]) * hi2f(w[i]);
      if (!h) n2 += lo2f(w2[0]) * lo2f(w2[0]) + hi2f(w2[0]) * hi2f(w2[0]) + lo2f(w2[1]) * lo2f(w2[1]) + hi2f(w2[1]) * hi2f(w2[1]);
      n2 += __shfl_xor(n2, 32);
      if (h) w2 = u32x2{__float_as_uint(__builtin_amdgcn_sqrtf(n2)), 0u};
      q1[t] = asb(w); qx2[t] = w2;
      *lp<u32x4>(L_QX + (wave * 2 + t) * 1536 + lane * 16) = w;
      *lp<u32x2>(L_QX + (wave * 2 + t) * 1536 + 1024 + lane * 8) = w2;
    }
    grp_bar(C_MAIN, 24, lane);
    u32x2 qy2[2];
#pragma unroll
    for (int t = 0; t < 2; t++) {
      q1[2 + t] = *lp<bf16x8>(L_QX + ((wave ^ 2) * 2 + t) * 1536 + lane * 16);
      qy2[t] = *lp<u32x2>(L_QX + ((wave ^ 2) * 2 + t) * 1536 + 1024 + lane * 8);
    }
    cnt_arrive(C_QDONE, lane);
    STAMP(3);
    u64 fv = flag_poll(P.ws, WS_F2 + lane * 8, true, tag);
    float km = lo2f((unsigned)fv & 0xffffu);
#pragma unroll
    for (int o = 16; o; o >>= 1) km = fmaxf(km, __shfl_xor(km, o));
    km = __uint_as_float(rfl(__float_as_uint(km))) * -1.01f;
#pragma unroll
    for (int t = 0; t < 4; t++) {
      u32x2 w2 = t < 2 ? qx2[t] : qy2[t - 2];
      if (h) w2[0] = pk2(__uint_as_float(w2[0]) * km, 0.f);
      q2[t] = __builtin_bit_cast(s16x4, w2);
    }
    STAMP(4);
  } else {
    const int j = wave - 4;
    const rsrc_t rk = mkrsrc(P.Ak, S * D * 2), rv = mkrsrc(P.Av, S * D * 2), rsh = mkrsrc(P.sh, D * 8), rh = mkrsrc(P.hk, D * S * 2);
#pragma unroll
    for (int i = 0; i < 3; i++) {
      const int p = j + 4 * i, u = p * 64 + lane, row = u / 10, ch = u - row * 10;
      const unsigned vo = (unsigned)((64 * rt + row) * 1280 + 160 * hg + 16 * ch);
      if (p < 11) dma16(rk, vo - 3840u, 0, L_KT + 1024 * p);
      if (p < 10) dma16(rv, vo, 0, L_VT + 1024 * p);
    }
    EPOCH_LOAD();
    if (j == 3) dma16(rsh, 640 * hg + 16 * lane, 0, L_SH);
#pragma unroll
    for (int c = 0; c < 3; c++) dma16(rh, c < nch ? (ch0 + c) * 4096 + j * 1024 + lane * 16 : OOB, 0, L_HB + c * HBC + 32 + j * 1024);
    if (j == 0 && lane < 6) *lp<u32x4>(L_HB + (lane >> 1) * HBC + (lane & 1) * 16) = u32x4{0, 0, 0, 0};
    EPOCH_WAIT();
    STAMP(14);
    __builtin_amdgcn_s_barrier();
    WAITV(3);
    STAMP(15);
    grp_bar(C_AUX, 4, lane);
    STAMP(1);
    for (int pass = j; pass < 10; pass += 4) {
      const int cg = pass >> 1, piece = 4 * (pass & 1) + g, rho = piece >> 1, half = piece & 1;
      const int s7 = 4 * (8 - 8 * half) + rho;
      const unsigned ka = L_KT + (s7 + 3 - tq) * 160 + cg * 32 + tp * 8;
      const unsigned va = L_VT + (s7 + 4 * (3 - tq)) * 160 + cg * 32 + tp * 8;
      const u32x2 wt = *lp<u32x2>(L_SH + (16 * cg + c16) * 8);
      const u32x2 v0 = trd(va + 2560), v1 = trd(va);
      float r[8];
#pragma unroll
      for (int e = 0; e < 8; e++) {
        const u32x2 kq = trd(ka + (7 - e) * 640);
        float t = dot2(kq[0], wt[0], dot2(kq[1], wt[1], 0.f));
        const unsigned vv = e < 4 ? v0[e >> 1] : v1[(e - 4) >> 1];
        r[e] = t * ((e & 1) ? hi2f(vv) : lo2f(vv));
      }
      __builtin_amdgcn_raw_buffer_store_b128(u32x4{pk2(r[0], r[1]), pk2(r[2], r[3]), pk2(r[4], r[5]), pk2(r[6], r[7])}, rws,
                                             WS_KVP + (80 * hg + 16 * cg + c16) * 4096 + rt * 128 + piece * 16, 0, SC1);
    }
    WAITV(0);
    if (cnt_arrive(C_F1, lane) == 3 && lane == 0) flag_store(P.ws, WS_F1 + (hg * 32 + rt) * 8, tag << 16);
    STAMP(2);
    flag_poll(P.ws, WS_F1 + (hg * 32 + col) * 8, true, tag);
    STAMP(3);
    {
      const int u = 64 * j + lane;
      u32x4 t[3];
#pragma unroll
      for (int c = 0; c < 3; c++) t[c] = __builtin_amdgcn_raw_buffer_load_b128(rws, c < nch ? WS_KVP + (ch0 + c) * 4096 + 16 * u : OOB, 0, SC1);
      if (lane < 24) *lp<u32x4>(L_XR + (3 * j + (lane >> 3)) * XRP + 1024 + (lane & 7) * 16) = u32x4{0, 0, 0, 0};
      const unsigned la = L_XR + ((u >> 1) & 3) * XRP + 2 * (496 - 16 * (u >> 3) + 8 * (u & 1));
#pragma unroll
      for (int c = 0; c < 3; c++) *lp<u32x4>(la + c * XRC) = t[c];
    }
    grp_bar(C_AUX, 8, lane);
    STAMP(4);
    float own[3][2][4] = {};
    const int srcl = (g ? lane - 16 : lane + 47) * 4;
#pragma unroll 1
    for (int c = 0; c < nch; c++) {
      const unsigned hb = L_HB + c * HBC + 8 + 64 * g + 8 * (tq + tp);
      const unsigned xb = L_XR + c * XRC + 1016 - 64 * 14 - 8 * c16 + 16 * g;
      float cur[2][4], spl[2][3];
      if (j == 0) conv_unit<0>(hb, xb, cur, spl);
      else if (j == 1) conv_unit<1>(hb, xb, cur, spl);
      else if (j == 2) conv_unit<2>(hb, xb, cur, spl);
      else conv_unit<3>(hb, xb, cur, spl);
#pragma unroll
      for (int z = 0; z < 2; z++) {
        const int t = z ? j : 7 - j;
        if (lane == 63 && t < 7) *lp<f32x4>(L_SPX + (c * 8 + t + 1) * 16) = f32x4{spl[z][0], spl[z][1], spl[z][2], 0.f};
#pragma unroll
        for (int r = 0; r < 3; r++) {
          float m = __int_as_float(__builtin_amdgcn_ds_bpermute(srcl, __float_as_int(spl[z][r])));
          cur[z][r] += lane ? m : 0.f;
        }
#pragma unroll
        for (int r = 0; r < 4; r++) {
#pragma unroll
          for (int cc = 0; cc < 3; cc++) own[cc][z][r] = c == cc ? cur[z][r] : own[cc][z][r];
        }
      }
    }
    grp_bar(C_AUX, 12, lane);
    STAMP(5);
#pragma unroll
    for (int c = 0; c < 3; c++) {
      if (c < nch) {
        const float dv = lo2f(P.Dv[ch0 + c]);
#pragma unroll
        for (int z = 0; z < 2; z++) {
          const int t = z ? j : 7 - j;
          const int p = 4 * c16 + g;
          if (lane == 0 && t > 0) { f32x4 e = *lp<f32x4>(L_SPX + (c * 8 + t) * 16); own[c][z][0] += e[0]; own[c][z][1] += e[1]; own[c][z][2] += e[2]; }
          float y[4];
#pragma unroll
          for (int r = 0; r < 4; r++) y[r] = own[c][z][r] + dv * lo2f(*lp<u16>(L_XR + c * XRC + r * XRP + 2 * (511 - 64 * t - p)));
          __builtin_amdgcn_raw_buffer_store_b64(u32x2{pk2(y[0], y[1]), pk2(y[2], y[3])}, rws, WS_ZT + (ch0 + c) * 4096 + 512 * t + 8 * p, 0, SC1);
        }
      }
    }
    WAITV(0);
    if (cnt_arrive(C_F3, lane) == 3 && lane == 0) flag_store(P.ws, WS_F3 + (hg * 32 + rt) * 8, tag << 16);
    STAMP(6);
    const rsrc_t rq = mkrsrc(P.Aq, S * D * 2), rwo = mkrsrc(P.wo, D * D * 2), rout = mkrsrc(P.out, S * D * 4);
    u32x4 qv[3];
#pragma unroll
    for (int i = 0; i < 3; i++) {
      const int it = 4 * (j + 4 * i) + g, rg = it / 10, cc = it - rg * 10;
      qv[i] = __builtin_amdgcn_raw_buffer_load_b128(rq, it < 40 ? (64 * rt + 16 * rg + c16) * 1280 + 160 * hg + 16 * cc : OOB, 0, 0);
    }
    flag_poll(P.ws, WS_F3 + (hg * 32 + col) * 8, true, tag);
    STAMP(7);
    {
      u32x4 t[3];
#pragma unroll
      for (int i = 0; i < 3; i++) {
        const int u = (j + 4 * i) * 64 + lane;
        t[i] = __builtin_amdgcn_raw_buffer_load_b128(rws, u < 640 ? WS_ZT + (80 * hg + (u >> 3)) * 4096 + rt * 128 + (u & 7) * 16 : OOB, 0, SC1);
      }
#pragma unroll
      for (int i = 0; i < 3; i++) { const int u = (j + 4 * i) * 64 + lane; if (u < 640) *lp<u32x4>(L_ZB + u * 16) = t[i]; }
    }
    grp_bar(C_AUX, 16, lane);
#pragma unroll
    for (int i = 0; i < 3; i++) {
      if (j + 4 * i < 10) {
        const int it = 4 * (j + 4 * i) + g, rg = it / 10, cc = it - rg * 10;
        const unsigned za = L_ZB + (8 * cc + tq) * 128 + rg * 32 + tp * 8;
        const u32x2 z0 = trd(za), z1 = trd(za + 512);
        u32x4 r;
        r[0] = pk2(lo2f(qv[i][0]) * lo2f(z0[0]), hi2f(qv[i][0]) * hi2f(z0[0]));
        r[1] = pk2(lo2f(qv[i][1]) * lo2f(z0[1]), hi2f(qv[i][1]) * hi2f(z0[1]));
        r[2] = pk2(lo2f(qv[i][2]) * lo2f(z1[0]), hi2f(qv[i][2]) * hi2f(z1[0]));
        r[3] = pk2(lo2f(qv[i][3]) * lo2f(z1[1]), hi2f(qv[i][3]) * hi2f(z1[1]));
        __builtin_amdgcn_raw_buffer_store_b128(r, rout, (64 * rt + 16 * rg + c16) * 2560 + 160 * hg + 16 * cc, 0, 0);
      }
    }
    cnt_wait(C_QDONE, 4);
    {
      unsigned vw[5];
#pragma unroll
      for (int i = 0; i < 5; i++) { int u = i * 64 + lane, pr = u / 10, ch = u - pr * 10; vw[i] = pr * 1280 + 160 * hg + 16 * ch; }
#pragma unroll
      for (int r = 0; r < 5; r++)
#pragma unroll
        for (int i = 0; i < 5; i++) dma16(rwo, vw[i], (5 * j + r) * 40960, L_WO + (5 * j + r) * 5120 + 1024 * i);
    }
    WAITV(0);
    STAMP(8);
  }

  for (int attempt = 0; attempt < 2; attempt++) {
    if (wave < 4) {
      const int rs = wave & 1, kh = wave >> 1;
      const unsigned vk = WS_IMG + lane * 16, vk2 = WS_IMG + K2OFF + (h ? 512 : col * 16);
      const int vrow = (col < 18) ? h * 20 + col : (col == 20 || col == 21) ? h * 20 + col - 2 : (col == 18 || col == 22) ? 40 : 41;
      const unsigned vv = WS_IMG + VOFF + vrow * 16;
      const f32x16 zero = {0};
      if (attempt) {
        float mx[4];
#pragma unroll
        for (int t = 0; t < 4; t++) { mx[t] = -3.0e38f; if (h) q2[t] = s16x4{0, 0, 0, 0}; }
#pragma unroll 1
        for (int r = 0; r < 64; r++) {
          const unsigned so = (r >> 1) * REC;
          u32x4 k1 = __builtin_amdgcn_raw_buffer_load_b128(rws, vk + (r & 1) * 1024, so, SC1);
          u32x2 k2 = __builtin_amdgcn_raw_buffer_load_b64(rws, vk2 + (r & 1) * 8, so, SC1);
#pragma unroll
          for (int t = 0; t < 4; t++) {
            f32x16 sv = MF32(asb(k1), q1[t], MF8(__builtin_bit_cast(s16x4, k2), q2[t], zero));
#pragma unroll
            for (int i = 0; i < 16; i++) mx[t] = fmaxf(mx[t], sv[i]);
          }
        }
#pragma unroll
        for (int t = 0; t < 4; t++) {
          const float m = -fmaxf(mx[t], __shfl_xor(mx[t], 32));
          const float m0 = hi2f(__float_as_uint(m)), m1 = hi2f(__float_as_uint(m - m0)), m2 = m - m0 - m1;
          if (h) q2[t] = __builtin_bit_cast(s16x4, u32x2{pk2(m0, m1), pk2(m2, 0.f)});
        }
      }
      Rec rA, rB;
      unsigned soff = kh * 16 * REC;
      loadrec(rA, rws, vk, vk2, vv, soff);
      f32x16 o[4] = {zero, zero, zero, zero};
      f32x16 s[3];
      s[2] = zero; s[1] = zero;
      rB.v[3] = u32x4{0, 0, 0, 0};
      int idx = 0xdddd;
      asm volatile("" : "+v"(idx));
      __builtin_amdgcn_s_waitcnt(vmenc(0));
      s[0] = MF32(asb(rA.k1[0]), q1[0], MF8(k2h<0>(rA.k2), q2[0], zero));
      STAMP(5);
#pragma unroll 1
      for (int it = 0; it < 8; it++) {
        soff += REC;
        dstep(rA, rB, s, o, q1, q2, idx, rws, vk, vk2, vv, soff);
        soff += REC;
        dstep(rB, rA, s, o, q1, q2, idx, rws, vk, vk2, vv, it == 7 ? 0u : soff);
      }
      o[3] = PV(asb(rB.v[3]), sub8<8>(s[2]), o[3]);
      STAMP(6);
      cnt_wait(C_F3, 4);
#pragma unroll
      for (int t = 0; t < 4; t++)
#pragma unroll
        for (int i = 0; i < 3; i++)
          *lp<f32x4>(L_OP + ((wave * 4 + ((2 * kh + t) & 3)) * 3 + i) * 1024 + lane * 16) = f32x4{o[t][4 * i], o[t][4 * i + 1], o[t][4 * i + 2], o[t][4 * i + 3]};
    }
    WAITL();
    __builtin_amdgcn_s_barrier();
    STAMP(9);
    {
      const int rs = wave & 1, hl = wave >> 1;
      const unsigned ox = WS_OX + ((rt * 8 + hg) * 2 + rs) * 5120;
      f32x4 r[3];
#pragma unroll
      for (int i = 0; i < 3; i++)
        r[i] = *lp<f32x4>(L_OP + ((rs * 4 + hl) * 3 + i) * 1024 + lane * 16) + *lp<f32x4>(L_OP + (((rs + 2) * 4 + hl) * 3 + i) * 1024 + lane * 16);
      const float inv = 1.0f / r[2][2];
      __builtin_amdgcn_raw_buffer_store_b128(u32x4{pk2(r[0][0] * inv, r[0][1] * inv), pk2(r[0][2] * inv, r[0][3] * inv), pk2(r[1][0] * inv, r[1][1] * inv), pk2(r[1][2] * inv, r[1][3] * inv)},
                                             rws, ox + hl * 1024 + lane * 16, 0, SC1);
      if (!(r[2][2] >= 9.0e-13f)) *lp<unsigned>(L_MISC + 4 * C_BAD) = 1;
      if (hl == 3) {
        u32x4 e;
#pragma unroll
        for (int t = 0; t < 4; t++) {
          f32x4 y = *lp<f32x4>(L_OP + ((rs * 4 + t) * 3 + 2) * 1024 + lane * 16) + *lp<f32x4>(L_OP + (((rs + 2) * 4 + t) * 3 + 2) * 1024 + lane * 16);
          const float iv = 1.0f / y[2];
          e[t] = pk2(y[0] * iv, y[1] * iv);
        }
        __builtin_amdgcn_raw_buffer_store_b128(e, rws, ox + 4096 + lane * 16, 0, SC1);
      }
    }
    WAITV(0);
    WAITL();
    __builtin_amdgcn_s_barrier();
    if (rfl(*(volatile LAS unsigned*)(size_t)(L_MISC + 4 * C_BAD)) == 0) break;
  }
  if (tid == 0) flag_store(P.ws, WS_F4 + (rt * 8 + hg) * 8, tag << 16);
  STAMP(10);

  {
    const int r16 = wave & 3, kh = wave >> 2;
    flag_poll(P.ws, WS_F4 + (rt * 8 + (lane & 7)) * 8, true, tag);
    STAMP(11);
    if (b == 0 && tid == 0) *(u64*)(P.ws + WS_EPOCH) = epoch;
    const unsigned ob = WS_OX + ((rt * 8 + 4 * kh) * 2 + (r16 >> 1)) * 5120 + ((g >> 1) * 32 + (r16 & 1) * 16 + c16) * 16;
    u32x4 of[10];
#pragma unroll
    for (int i = 0; i < 8; i++) of[i] = __builtin_amdgcn_raw_buffer_load_b128(rws, ob + (g & 1) * 1024, (i >> 1) * 10240 + (i & 1) * 2048, SC1);
#pragma unroll
    for (int i = 0; i < 2; i++) of[8 + i] = __builtin_amdgcn_raw_buffer_load_b128(rws, ob + (g & 1) * 10240 + 4096, i * 20480, SC1);
    const unsigned wm = L_WO + (320 * kh + 20 * (g & 1) + 8 * (g >> 1) + tq) * 160 + tp * 8;
    const unsigned wr = L_WO + (320 * kh + 80 * (g & 1) + 20 * (tq >> 1) + 16 + 2 * (g >> 1) + (tq & 1)) * 160 + tp * 8;
    f32x4 acc[5];
#pragma unroll
    for (int m = 0; m < 5; m++) acc[m] = f32x4{0, 0, 0, 0};
#pragma unroll
    for (int i = 0; i < 10; i++) {
#pragma unroll
      for (int m = 0; m < 5; m++) {
        bf16x8 a;
        if (i < 8) a = cat2(trd(wm + i * 6400 + m * 32), trd(wm + i * 6400 + m * 32 + 640));
        else a = cat2(trd(wr + (i - 8) * 25600 + m * 32), trd(wr + (i - 8) * 25600 + m * 32 + 6400));
        acc[m] = MF16(a, asb(of[i]), acc[m]);
      }
    }
    STAMP(12);
    if (kh) {
#pragma unroll
      for (int m = 0; m < 5; m++) *lp<f32x4>(L_RED + (r16 * 5 + m) * 1024 + lane * 16) = acc[m];
    }
    WAITL();
    __builtin_amdgcn_s_barrier();
    if (!kh) {
      const rsrc_t rout = mkrsrc(P.out, S * D * 4);
#pragma unroll
      for (int m = 0; m < 5; m++) {
        f32x4 y = acc[m] + *lp<f32x4>(L_RED + (r16 * 5 + m) * 1024 + lane * 16);
        __builtin_amdgcn_raw_buffer_store_b64(u32x2{pk2(y[0], y[1]), pk2(y[2], y[3])}, rout, (64 * rt + 16 * r16 + c16) * 2560 + 1280 + 160 * hg + 32 * m + 8 * g, 0, 0);
      }
    }
    STAMP(13);
    STAMP_OUT();
  }
}

extern "C" void kernel_launch(void* const* d_in, const int* in_sizes, int n_in,
                              void* d_out, int out_size, void* d_ws, size_t ws_size,
                              hipStream_t stream) {
  Params P;
  P.Aq = (const u16*)d_in[0]; P.Ak = (const u16*)d_in[1]; P.Av = (const u16*)d_in[2];
  P.sh = (const u16*)d_in[3]; P.hk = (const u16*)d_in[4]; P.Dv = (const u16*)d_in[5];
  P.x = (const u16*)d_in[6]; P.wq = (const u16*)d_in[7]; P.wk = (const u16*)d_in[8];
  P.wv = (const u16*)d_in[9]; P.wo = (const u16*)d_in[10];
  P.out = (u16*)d_out; P.ws = (char*)d_ws;
  (void)hipFuncSetAttribute((const void*)fused_kernel, hipFuncAttributeMaxDynamicSharedMemorySize, LDS_BYTES);
  fused_kernel<<<256, 512, LDS_BYTES, stream>>>(P);
}
```

```cpp
#include <hip/hip_runtime.h>
#include <hip/hip_bf16.h>

typedef unsigned short u16;
typedef unsigned long long u64;
typedef __attribute__((ext_vector_type(16))) float f32x16;
typedef __attribute__((ext_vector_type(8))) float f32x8;
typedef __attribute__((ext_vector_type(4))) float f32x4;
typedef __attribute__((ext_vector_type(16))) __bf16 bf16x16;
typedef __attribute__((ext_vector_type(8))) __bf16 bf16x8;
typedef __attribute__((ext_vector_type(2))) __bf16 bf16x2;
typedef __attribute__((ext_vector_type(4))) short s16x4;
typedef __attribute__((ext_vector_type(4))) unsigned u32x4;
typedef __attribute__((ext_vector_type(2))) unsigned u32x2;
typedef __amdgpu_buffer_rsrc_t rsrc_t;
#define LAS __attribute__((address_space(3)))
#define GAS __attribute__((address_space(1)))
#define DI __device__ __forceinline__
#define SB() __builtin_amdgcn_sched_barrier(0)

constexpr int S = 2048, D = 640;
constexpr float KSCALE = 0.22360679775f * 1.44269504089f;

constexpr unsigned WS_EPOCH = 0;
constexpr unsigned WS_F1 = 65536;
constexpr unsigned WS_F2 = 131072;
constexpr unsigned WS_F3 = 196608;
constexpr unsigned WS_F4 = 262144;
constexpr unsigned WS_KVP = 1u << 20;
constexpr unsigned WS_IMG = 4u << 20;
constexpr unsigned WS_ZT = 5u << 20;
constexpr unsigned WS_OX = 8u << 20;
constexpr unsigned WS_BYTES = 12u << 20;
constexpr unsigned REC = 5376, K2OFF = 2048, K2S = 320, VOFF = 2688, VFR = 672;

constexpr unsigned L_X = 0, L_WKV = 81920, L_WQ = 107520, SLOT = 10240, L_QX = L_WKV, L_KVR = 158720;
constexpr unsigned L_HB = 81920, HCS = 4160, HBC = 4 * HCS;
constexpr unsigned L_KT = 131840, L_VT = L_KT + 11264, L_SH = L_KT + 21504;
constexpr unsigned L_XR = L_KT, XRC = 4608;
constexpr unsigned L_WO = 0, L_OP = 102400, L_ZB = 151552, L_RED = L_OP;
constexpr unsigned L_MISC = 163328;
constexpr unsigned LDS_BYTES = 163840;
enum { C_MAIN = 0, C_AUX = 1, C_F1 = 2, C_F3 = 4, C_KVD = 5, C_QDONE = 6, C_KVP = 7, C_BAD = 8, C_LA = 9, C_LAND = 10, W_TAG = 12, C_KM = 14, W_KM = 15, C_FREE = 16, C_P1 = 20, C_P3 = 21, C_AI = 22, C_LI = 23 };
#define HOLD 8
#define LSPLIT 0
#define TLBW 0
#define PSL 3
#define HSL 3
#define PACE 1

constexpr unsigned CODE_SPAN = 16384;
constexpr int SC1 = 16;
constexpr unsigned OOB = 0x80000000u;
constexpr unsigned SPIN_LDS = 1u << 20, SPIN_GLB = 1u << 17;

struct Params { const u16 *Aq, *Ak, *Av, *sh, *hk, *Dv, *x, *wq, *wk, *wv, *wo; u16* out; char* ws; };

DI float lo2f(unsigned v) { return __uint_as_float(v << 16); }
DI float hi2f(unsigned v) { return __uint_as_float(v & 0xffff0000u); }
DI unsigned pk2(float a, float b) { bf16x2 t = {(__bf16)a, (__bf16)b}; return __builtin_bit_cast(unsigned, t); }
DI float dot2(unsigned a, unsigned b, float c) { return __builtin_amdgcn_fdot2_f32_bf16(__builtin_bit_cast(bf16x2, a), __builtin_bit_cast(bf16x2, b), c, false); }
DI rsrc_t mkrsrc(const void* p, unsigned n) { return __builtin_amdgcn_make_buffer_rsrc((void*)p, 0, n, 0x00020000); }
DI void dma16(rsrc_t rs, unsigned voff, unsigned soff, unsigned lds) {
  asm volatile("s_mov_b32 m0, %0\n\ts_nop 0\n\tbuffer_load_dwordx4 %1, %2, %3 offen lds" :: "s"(lds), "v"(voff), "s"(rs), "s"(soff) : "memory");
}
#define WAITV(n) asm volatile("s_waitcnt vmcnt(" #n ")" ::: "memory")
#define WAITL() asm volatile("s_waitcnt lgkmcnt(0)" ::: "memory")
template <typename T> DI LAS T* lp(unsigned a) { return (LAS T*)(size_t)a; }
DI u32x2 trd(unsigned a) { return __builtin_bit_cast(u32x2, __builtin_amdgcn_ds_read_tr16_b64_v4i16(lp<s16x4>(a))); }
DI bf16x8 cat2(u32x2 a, u32x2 b) { return __builtin_bit_cast(bf16x8, __builtin_shufflevector(a, b, 0, 1, 2, 3)); }
DI bf16x8 asb(u32x4 v) { return __builtin_bit_cast(bf16x8, v); }
DI unsigned rfl(unsigned v) { return __builtin_amdgcn_readfirstlane(v); }

DI unsigned cnt_arrive(int c, int lane) {
  unsigned r; u64 save;
  asm volatile("s_waitcnt lgkmcnt(0)\n\ts_mov_b64 %1, exec\n\ts_mov_b64 exec, 1\n\tds_add_rtn_u32 %0, %2, %3\n\ts_mov_b64 exec, %1\n\ts_waitcnt lgkmcnt(0)"
               : "=&v"(r), "=&s"(save) : "v"(L_MISC + 4 * c), "v"(1u) : "memory");
  return rfl(r);
}
DI void cnt_signal(int c) {
  u64 save;
  asm volatile("s_waitcnt lgkmcnt(0)\n\ts_mov_b64 %0, exec\n\ts_mov_b64 exec, 1\n\tds_add_u32 %1, %2\n\ts_mov_b64 exec, %0"
               : "=&s"(save) : "v"(L_MISC + 4 * c), "v"(1u) : "memory");
}
template <int SLP = 2> DI void cnt_wait(int c, unsigned target) {
#pragma nounroll
  for (unsigned spins = 0; spins < SPIN_LDS; ++spins) {
    unsigned v = __hip_atomic_load(lp<unsigned>(L_MISC + 4 * c), __ATOMIC_RELAXED, __HIP_MEMORY_SCOPE_WORKGROUP);
    if (rfl(v) >= target) break;
    __builtin_amdgcn_s_sleep(SLP);
  }
  asm volatile("" ::: "memory");
}
DI void cnt_wait2(int c, unsigned target) {
#pragma nounroll
  for (unsigned spins = 0; spins < SPIN_LDS; ++spins) {
    u32x2 v = *(volatile LAS u32x2*)(size_t)(L_MISC + 4 * c);
    if (rfl(min(v[0], v[1])) >= target) break;
    __builtin_amdgcn_s_sleep(1);
  }
  asm volatile("" ::: "memory");
}
DI void cnt_wait4(int c, unsigned target) {
#pragma nounroll
  for (unsigned spins = 0; spins < SPIN_LDS; ++spins) {
    u32x4 v = *(volatile LAS u32x4*)(size_t)(L_MISC + 4 * c);
    if (rfl(min(min(v[0], v[1]), min(v[2], v[3]))) >= target) break;
    __builtin_amdgcn_s_sleep(1);
  }
  asm volatile("" ::: "memory");
}
DI void prog_set(int c, unsigned v) { asm volatile("" ::: "memory"); *(volatile LAS unsigned*)(size_t)(L_MISC + 4 * c) = v; asm volatile("" ::: "memory"); }
DI void grp_bar(int c, unsigned target, int lane) { if (cnt_arrive(c, lane) + 1 < target) cnt_wait<4>(c, target); asm volatile("" ::: "memory"); }

DI void flag_store(char* ws, unsigned off, u64 v) { __hip_atomic_store((GAS u64*)(ws + off), v, __ATOMIC_RELAXED, __HIP_MEMORY_SCOPE_AGENT); }
template <int SLP> DI u64 flag_poll(char* ws, unsigned off, u64 tag) {
  u64 v = 0;
#pragma nounroll
  for (unsigned spins = 0; spins < SPIN_GLB; ++spins) {
    v = __hip_atomic_load((GAS u64*)(ws + off), __ATOMIC_RELAXED, __HIP_MEMORY_SCOPE_AGENT);
    if (__all((v >> 16) == tag)) break;
    __builtin_amdgcn_s_sleep(SLP);
  }
  __builtin_amdgcn_fence(__ATOMIC_ACQUIRE, "wavefront");
  asm volatile("" ::: "memory");
  return v;
}

DI u64 flag_poll2(char* ws, unsigned off, u64 tag) {
  u64 v = 0;
#pragma nounroll
  for (unsigned spins = 0; spins < SPIN_GLB; ++spins) {
    v = __hip_atomic_load((GAS u64*)(ws + off), __ATOMIC_RELAXED, __HIP_MEMORY_SCOPE_AGENT);
    u64 w = __hip_atomic_load((GAS u64*)(ws + off + 512), __ATOMIC_RELAXED, __HIP_MEMORY_SCOPE_AGENT);
    if (__all((v >> 16) == tag && (w >> 16) == tag)) break;
    __builtin_amdgcn_s_sleep(2);
  }
  __builtin_amdgcn_fence(__ATOMIC_ACQUIRE, "wavefront");
  asm volatile("" ::: "memory");
  return v;
}

#define MF32(a, b, c) __builtin_amdgcn_mfma_f32_32x32x16_bf16(a, b, c, 0, 0, 0)
#define MF8(a, b, c) __builtin_amdgcn_mfma_f32_32x32x8bf16_1k(a, b, c, 0, 0, 0)
#define MF16(a, b, c) __builtin_amdgcn_mfma_f32_16x16x32_bf16(a, b, c, 0, 0, 0)
#define PV(v, p, o) __builtin_amdgcn_smfmac_f32_32x32x32_bf16(v, p, o, idx, 0, 0)

#define STAMP(i)
#define STAMPI(i)
#define STAMP_DECL
#define STAMP_OUT()

template <int O> DI bf16x16 sub8(f32x16 v) {
  f32x8 t = __builtin_shufflevector(v, v, O, O + 1, O + 2, O + 3, O + 4, O + 5, O + 6, O + 7);
  return __builtin_bit_cast(bf16x16, t);
}
template <int A, int B> DI void exps(f32x16& s) {
#pragma unroll
  for (int i = A; i < B; i++) s[i] = __builtin_amdgcn_exp2f(s[i]);
}
DI s16x4 as4(u32x2 v) { return __builtin_bit_cast(s16x4, v); }
struct Rec { u32x4 k1[2]; u32x2 k2[2]; u32x4 v[4]; };
constexpr int vmenc(int n) { return (n & 15) | ((n >> 4) << 14) | (7 << 4) | (15 << 8); }

template <typename F4, typename F6>
DI void tile(f32x16& sc, f32x16& sprev, f32x16& oprev, f32x16& ocur, bf16x8 vprev_b, bf16x8 vcur_a,
             bf16x8 kn1, s16x4 kn2, bf16x8 qn1, s16x4 qn2, f32x16& sn, int idx, F4 f4, F6 f6) {
  const f32x16 zero = {0};
  SB(); f32x16 t = MF8(kn2, qn2, zero);
  SB(); exps<0, 4>(sc);
  SB(); t = MF32(kn1, qn1, t);
  SB(); exps<4, 8>(sc);
  SB(); oprev = PV(vprev_b, sub8<8>(sprev), oprev);
  SB(); f4();
  SB(); exps<8, 12>(sc);
  SB(); ocur = PV(vcur_a, sub8<0>(sc), ocur);
  SB(); f6();
  SB(); exps<12, 16>(sc);
  SB(); sn = t;
}
#define LD16(off, so) __builtin_amdgcn_raw_buffer_load_b128(rs, off, so, SC1)
#define LD8(off, so) __builtin_amdgcn_raw_buffer_load_b64(rs, off, so, SC1)
template <int PART> DI void loadrec(Rec& a, Rec& r, rsrc_t rs, unsigned vk, unsigned vk2, unsigned vv, unsigned soff) {
  if (PART == 0) {
    SB(); a.k2[0] = LD8(vk2, soff); SB(); a.k1[0] = LD16(vk, soff); SB(); a.v[0] = LD16(vv, soff); SB(); a.v[1] = LD16(vv + VFR, soff); SB();
  } else {
    SB(); a.k2[1] = LD8(vk2 + K2S, soff); SB(); a.k1[1] = LD16(vk + 1024, soff); SB(); a.v[2] = LD16(vv + 2 * VFR, soff); SB(); a.v[3] = LD16(vv + 3 * VFR, soff);
    soff += REC;
    SB(); r.k2[0] = LD8(vk2, soff); SB(); r.k1[0] = LD16(vk, soff); SB(); r.v[0] = LD16(vv, soff); SB(); r.v[1] = LD16(vv + VFR, soff);
    SB(); r.k2[1] = LD8(vk2 + K2S, soff); SB(); r.k1[1] = LD16(vk + 1024, soff); SB(); r.v[2] = LD16(vv + 2 * VFR, soff); SB();
  }
}
DI void dstep(Rec& c, Rec& n, f32x16 (&s)[3], f32x16 (&o)[4], const bf16x8 (&q1)[4], const s16x4 (&q2)[4], int idx,
              rsrc_t rs, unsigned vk, unsigned vk2, unsigned vv, unsigned s1, unsigned s2) {
  auto N = []() {};
#define L_(dst, expr) [&]() { dst = expr; }
#define W_(n) []() { __builtin_amdgcn_s_waitcnt(vmenc(n)); }
#define LW_(dst, expr, n) [&]() { dst = expr; SB(); __builtin_amdgcn_s_waitcnt(vmenc(n)); }
  tile(s[0], s[2], o[3], o[0], asb(n.v[3]), asb(c.v[0]), asb(c.k1[0]), as4(c.k2[0]), q1[1], q2[1], s[1], idx, L_(n.v[3], LD16(vv + 3 * VFR, s1)), W_(12));
  tile(s[1], s[0], o[0], o[1], asb(c.v[1]), asb(c.v[0]), asb(c.k1[0]), as4(c.k2[0]), q1[2], q2[2], s[2], idx, N, N);
  tile(s[2], s[1], o[1], o[2], asb(c.v[1]), asb(c.v[0]), asb(c.k1[0]), as4(c.k2[0]), q1[3], q2[3], s[0], idx, L_(c.k2[0], LD8(vk2, s2)), LW_(c.k1[0], LD16(vk, s2), 12));
  tile(s[0], s[2], o[2], o[3], asb(c.v[1]), asb(c.v[0]), asb(c.k1[1]), as4(c.k2[1]), q1[0], q2[0], s[1], idx, N, LW_(c.v[0], LD16(vv, s2), 12));
  tile(s[1], s[0], o[3], o[0], asb(c.v[1]), asb(c.v[2]), asb(c.k1[1]), as4(c.k2[1]), q1[1], q2[1], s[2], idx, L_(c.v[1], LD16(vv + VFR, s2)), W_(12));
  tile(s[2], s[1], o[0], o[1], asb(c.v[3]), asb(c.v[2]), asb(c.k1[1]), as4(c.k2[1]), q1[2], q2[2], s[0], idx, N, N);
  tile(s[0], s[2], o[1], o[2], asb(c.v[3]), asb(c.v[2]), asb(c.k1[1]), as4(c.k2[1]), q1[3], q2[3], s[1], idx, L_(c.k2[1], LD8(vk2 + K2S, s2)), LW_(c.k1[1], LD16(vk + 1024, s2), 12));
  tile(s[1], s[0], o[2], o[3], asb(c.v[3]), asb(c.v[2]), asb(n.k1[0]), as4(n.k2[0]), q1[0], q2[0], s[2], idx, N, LW_(c.v[2], LD16(vv + 2 * VFR, s2), 12));
  f32x16 a = s[2], bb = s[1]; s[0] = a; s[2] = bb;
#undef L_
#undef W_
}

template <int PH> DI void pace() {
  SB();
  if (PACE == 1) asm volatile("s_nop 3");
  if (PACE == 2) asm volatile("s_nop 1");
  if (PACE == 3) asm volatile("s_nop 0");
  if (PACE == 4 && PH == 0) asm volatile("s_nop 1");
  if (PACE == 5 && PH == 0) asm volatile("s_nop 3");
  SB();
}
DI bf16x8 lda(unsigned a) { u32x2 x = *(volatile LAS u32x2*)(size_t)a, y = *(volatile LAS u32x2*)(size_t)(a + 8); return cat2(x, y); }
template <typename F> DI void conv_pair(unsigned pa, unsigned pb, int t, f32x4& acc1, f32x4& acc0, F hook) {
  bf16x8 A[8], B[8];
#pragma unroll
  for (int e = 0; e < 8; e++) { A[e] = lda(pa + 64 * e); B[e] = *lp<bf16x8>(pb + 64 * e); }
#pragma unroll 1
  for (int m = 0; m < t; m++) {
#pragma unroll
    for (int e = 0; e < 8; e++) { SB(); acc1 = MF16(A[e], B[e], acc1); SB(); B[e] = *lp<bf16x8>(pb + 512 + 64 * e); pace<0>(); }
#pragma unroll
    for (int e = 0; e < 8; e++) { SB(); acc0 = MF16(A[e], B[e], acc0); SB(); A[e] = lda(pa + 512 + 64 * e); pace<1>(); }
    pa += 512; pb += 512;
    hook();
  }
#pragma unroll
  for (int e = 0; e < 8; e++) { SB(); acc1 = MF16(A[e], B[e], acc1); asm volatile("s_nop 7"); }
}

template <bool SWAP>
DI void kv_half(f32x16& acc, const unsigned (&xa)[4], unsigned wa) {
  bf16x8 xf[2][4], wf[2][4];
#pragma unroll
  for (int i = -1; i < 5; i++) {
    const int l = i + 1;
    if (l < 5) {
      if (l == 0) WAITV(28); else if (l == 1) WAITV(21); else if (l == 2) WAITV(14); else if (l == 3) WAITV(7); else { WAITV(0); cnt_signal(C_LA); }
#pragma unroll
      for (int k = 0; k < 4; k++) {
        xf[l & 1][k] = *lp<bf16x8>(xa[k] + l * 8192);
        wf[l & 1][k] = cat2(trd(wa + l * 2560 + k * 640), trd(wa + l * 2560 + k * 640 + 160));
      }
    }
    SB();
    if (i >= 0) {
#pragma unroll
      for (int k = 0; k < 4; k++) acc = SWAP ? MF32(xf[i & 1][k], wf[i & 1][k], acc) : MF32(wf[i & 1][k], xf[i & 1][k], acc);
    }
    SB();
  }
}
DI float xhalf_sum(float v) {
  auto r = __builtin_amdgcn_permlane32_swap(__float_as_uint(v), __float_as_uint(v), false, false);
  return __uint_as_float(r[0]) + __uint_as_float(r[1]);
}

__global__ __launch_bounds__(512, 2) void fused_kernel(Params P) {
  extern __shared__ __attribute__((aligned(16))) char smem_raw[];
  const int tid = threadIdx.x, lane = tid & 63;
  const int wave = rfl(tid >> 6);
  const int b = blockIdx.x;
  const int rt = 4 * (b & 7) + (b >> 6), hg = (b >> 3) & 7;
  const bool prod = !((rt + hg) & 1), isV = hg >= 4;
  const int prs = (hg >> 1) & 1;
  const int col = lane & 31, h = lane >> 5, g = lane >> 4, c16 = lane & 15, tq = (lane & 15) >> 2, tp = lane & 3;
  const rsrc_t rws = mkrsrc(P.ws, WS_BYTES);
  asm volatile("" :: "v"(smem_raw));
  STAMP_DECL;
  STAMP(0);
  if (tid < 32) *lp<unsigned>(L_MISC + 4 * tid) = 0;
  asm volatile("" :: "s"(P.Ak), "s"(P.x), "s"(P.wq), "s"(P.wk), "s"(P.ws));
  WAITL();
  __builtin_amdgcn_s_barrier();
  u64 tag;
  bf16x8 q1[4]; s16x4 q2[4];
  Rec rA, rB;
  unsigned vq[3], zl[3]; u32x4 qv[3];
  f32x16 o[4];
  const int j = wave - 4;
  const int cc0 = (hg + (b >> 6)) % 5;
  const unsigned vk = WS_IMG + lane * 16, vk2 = WS_IMG + K2OFF + (h ? 256 : col * 8);
  const int vrow = (col < 18) ? h * 20 + col : (col == 20 || col == 21) ? h * 20 + col - 2 : (col == 18 || col == 22) ? 40 : 41;
  const unsigned vv = WS_IMG + VOFF + vrow * 16;
  const unsigned wpc = (g & 1) ? 32 : ((tp & 1) * 2 + (tp >> 1)) * 8;

  if (wave >= 4) {
    const rsrc_t rx = mkrsrc(P.x, S * D * 2);
    unsigned vx[2];
#pragma unroll
    for (int i = 0; i < 2; i++) { int r7 = lane >> 3, pos = lane & 7; vx[i] = (64 * rt + 8 * i + r7) * 1280 + 16 * (pos ^ (4 * i + (r7 >> 1))); }
    auto xhalf = [&](int r, int hc) {
      const unsigned sx = 128 * hc + r * 40960, lx = L_X + hc * 8192 + r * 4096;
      dma16(rx, vx[0], sx, lx); dma16(rx, vx[1], sx, lx + 1024);
      dma16(rx, vx[0], sx + 20480, lx + 2048); dma16(rx, vx[1], sx + 20480, lx + 3072);
    };
    if (j < 2) {
      const rsrc_t rwq = mkrsrc(P.wq, D * D * 2);
      unsigned vw[5];
#pragma unroll
      for (int i = 0; i < 5; i++) { unsigned u = i * 64 + lane, pr = (u * 6554u) >> 16, ch = u - pr * 10, kk = (pr & ~7u) + ((pr & 1) << 2) + ((pr >> 1) & 3); vw[i] = kk * 1280 + 160 * hg + 16 * ch; }
      int hc = 2 * cc0 + j;
      if (prod) cnt_wait<1>(C_AI, 2);
      STAMP(7);
#pragma unroll 1
      for (int c = j; c < 10; c += 2) {
        if (c >= 5) cnt_wait4(C_FREE, c - 4);
        const unsigned slot = L_WQ + (c >= 5 ? c - 5 : c) * SLOT;
        if (!prod) xhalf(prs, hc);
        xhalf(prs ^ 1, hc);
#pragma unroll
        for (int i = 0; i < 5; i++) dma16(rwq, vw[i], 81920 * hc, slot + 1024 * i);
#pragma unroll
        for (int i = 0; i < 5; i++) dma16(rwq, vw[i], 81920 * hc + 40960, slot + 5120 + 1024 * i);
        if (c >= 2) {
          if (prod) WAITV(14); else WAITV(18);
          prog_set(C_LAND + j, c >> 1);
        }
        hc = hc >= 8 ? hc - 8 : hc + 2;
      }
      cnt_signal(C_LI);
      WAITV(0);
      prog_set(C_LAND + j, 5);
      STAMP(8);
      cnt_wait(C_KM, 1);
      tag = ((u64)rfl(*lp<unsigned>(L_MISC + 4 * W_TAG + 4)) << 32) | rfl(*lp<unsigned>(L_MISC + 4 * W_TAG));
    } else {
      __builtin_amdgcn_s_setprio(3);
      const int jj = j - 2;
      if (prod) {
        const rsrc_t rwkv = mkrsrc(isV ? P.wv : P.wk, D * 40);
#pragma unroll 1
        for (int i = 5 * jj; i < 5 * jj + 5; i++) {
          xhalf(prs, i);
          dma16(rwkv, 16 * lane, 2560 * i, L_WKV + 2560 * i);
          dma16(rwkv, 16 * lane, 2560 * i + 1024, L_WKV + 2560 * i + 1024);
          if (lane < 32) dma16(rwkv, 16 * lane, 2560 * i + 2048, L_WKV + 2560 * i + 2048);
        }
        cnt_signal(C_AI);
      } else if (jj == 0) {
        u64 pc; asm volatile("s_getpc_b64 %0" : "=s"(pc));
        const unsigned o = ((b >> 4) * 16 + c16) * 128;
        if (lane < 16 && o < CODE_SPAN) { unsigned t; asm volatile("global_load_dword %0, %1, %2\n\ts_waitcnt vmcnt(0)" : "=&v"(t) : "v"(o), "s"(pc & ~127ull) : "memory"); }
      }
      u64 epoch;
      asm volatile("s_load_dwordx2 %0, %1, 0x0\n\ts_waitcnt lgkmcnt(0)" : "=s"(epoch) : "s"(P.ws) : "memory");
      tag = (epoch + 1) & 0xffffffffffffull;
      if (prod) {
        const int xr = isV ? (col & 19) | ((col & 4) << 1) | ((col & 8) >> 1) : col;
        unsigned xa[4];
#pragma unroll
        for (int i = 0; i < 4; i++) xa[i] = L_X + jj * 40960 + (4 * prs + (xr >> 3)) * 1024 + (xr & 7) * 128 + 16 * ((2 * i + h) ^ ((xr >> 1) & 7));
        const unsigned wa = L_WKV + jj * 12800 + (8 * h + tq) * 40 + wpc;
        f32x16 acc = {0};
        if (isV) kv_half<true>(acc, xa, wa); else kv_half<false>(acc, xa, wa);
        cnt_signal(C_KVD);
        if (!jj) STAMP(7);
        if (jj) {
#pragma unroll
          for (int i = 0; i < 4; i++) *lp<f32x4>(L_KVR + i * 1024 + lane * 16) = f32x4{acc[4 * i], acc[4 * i + 1], acc[4 * i + 2], acc[4 * i + 3]};
          cnt_signal(C_KVP);
        } else {
          cnt_wait<0>(C_KVP, 1);
#pragma unroll
          for (int i = 0; i < 4; i++) { f32x4 y = *lp<f32x4>(L_KVR + i * 1024 + lane * 16); acc[4 * i] += y[0]; acc[4 * i + 1] += y[1]; acc[4 * i + 2] += y[2]; acc[4 * i + 3] += y[3]; }
          const unsigned rec = WS_IMG + rt * REC;
          unsigned kb = 0;
          if (!isV) {
            u32x4 w = {pk2(acc[0] * KSCALE, acc[1] * KSCALE), pk2(acc[2] * KSCALE, acc[3] * KSCALE), pk2(acc[4] * KSCALE, acc[5] * KSCALE), pk2(acc[6] * KSCALE, acc[7] * KSCALE)};
            u32x2 w2 = {pk2(acc[8] * KSCALE, acc[9] * KSCALE), pk2(acc[10] * KSCALE, acc[11] * KSCALE)};
            __builtin_amdgcn_raw_buffer_store_b128(w, rws, rec + prs * 1024 + lane * 16, 0, SC1);
            __builtin_amdgcn_raw_buffer_store_b64(h ? u32x2{0x3f803f80u, 0x00003f80u} : w2, rws, lane <= 32 ? rec + K2OFF + prs * K2S + lane * 8 : OOB, 0, SC1);
            float n2 = xhalf_sum(dot2(w[0], w[0], dot2(w[1], w[1], dot2(w[2], w[2], dot2(w[3], w[3], 0.f)))));
            n2 = dot2(w2[0], w2[0], dot2(w2[1], w2[1], n2));
#pragma unroll
            for (int o = 16; o; o >>= 1) n2 = __uint_as_float(max(__float_as_uint(n2), __float_as_uint(__shfl_xor(n2, o))));
            kb = (__float_as_uint(__builtin_amdgcn_sqrtf(n2)) + 0xffffu) >> 16;
          } else {
            const unsigned cv = col == 20 ? 0x3f803f80u : 0u;
            const unsigned uo = col < 20 ? (h * 20 + col) * 16 : (col < 22 && !h) ? (20 + col) * 16 : OOB;
#pragma unroll
            for (int f = 0; f < 2; f++) {
              u32x4 w = {pk2(acc[8 * f], acc[8 * f + 1]), pk2(acc[8 * f + 2], acc[8 * f + 3]), pk2(acc[8 * f + 4], acc[8 * f + 5]), pk2(acc[8 * f + 6], acc[8 * f + 7])};
              if (col >= 20) w = u32x4{cv, cv, cv, cv};
              __builtin_amdgcn_raw_buffer_store_b128(w, rws, uo, rec + VOFF + (2 * prs + f) * VFR, SC1);
            }
          }
          STAMP(8);
          WAITV(0);
          if (lane < 32) flag_store(P.ws, WS_F2 + lane * 1024 + ((hg >> 1) * 32 + rt) * 8, (tag << 16) | kb);
          STAMP(14);
        }
      }
      if (jj) {
        cnt_wait<1>(C_LI, 2);
        STAMP(7);
        u64 fv = flag_poll2(P.ws, WS_F2 + (b & 31) * 1024 + lane * 8, tag);
        asm volatile("ds_max_u32 %0, %1" :: "v"(L_MISC + 4 * W_KM), "v"((unsigned)fv << 16) : "memory");
        *lp<u32x2>(L_MISC + 4 * W_TAG) = u32x2{(unsigned)tag, (unsigned)(tag >> 32)};
        cnt_signal(C_KM);
        STAMP(8);
      }
    }
    const int cb = 80 * hg + 5 * (rt >> 1);
    const int chs[3] = {cb + 3 * (rt & 1), cb + 3 * (rt & 1) + 1, cb + 2};
    const rsrc_t rk = mkrsrc(P.Ak, S * D * 2), rv = mkrsrc(P.Av, S * D * 2), rsh = mkrsrc(P.sh, D * 8), rh = mkrsrc(P.hk, D * S * 2);
    const rsrc_t rq = mkrsrc(P.Aq, S * D * 2), rwo = mkrsrc(P.wo, D * D * 2);
    unsigned vp[3], vwo[5];
#pragma unroll
    for (int i = 0; i < 3; i++) {
      const unsigned u = (j + 4 * i) * 64 + lane, row = (u * 6554u) >> 16, ch = u - row * 10;
      vp[i] = (64 * rt + row) * 1280 + 160 * hg + 16 * ch;
      const unsigned it = 4 * (j + 4 * i) + g, rg = (it * 6554u) >> 16, cc = it - rg * 10;
      vq[i] = it < 40 ? (64 * rt + 16 * rg + c16) * 1280 + 16 * cc : OOB;
      zl[i] = L_ZB + (8 * cc + tq) * 128 + rg * 32 + tp * 8;
    }
#pragma unroll
    for (int i = 0; i < 5; i++) { unsigned u = i * 64 + lane, pr = (u * 6554u) >> 16, ch = u - pr * 10; vwo[i] = (32 * j + pr) * 1280 + 160 * hg + 16 * ch; }
    const int oc = 4 * (j & 1) + g;
    unsigned ka = L_KT + (8 * oc + 5 - tq) * 160 + (j >> 1) * 32 + tp * 8;
    unsigned kx = L_KT + (8 * oc + 2 - ((tq + 1) >> 1)) * 160 + (j >> 1) * 32 + tp * 8;
    unsigned wl = L_SH + (16 * (j >> 1) + c16) * 8;
    unsigned so = WS_KVP + (80 * hg + 16 * (j >> 1) + c16) * 4096 + (31 - rt) * 128 + (7 - oc) * 16;
    unsigned la = L_HB + ((c16 + 1) & 3) * HCS + 16 * g + 8 * ((c16 + 1) >> 2);
    unsigned lb = L_XR + 4064 - 32 * c16 + 16 * g;
    unsigned ld = L_XR + 4088 - 32 * c16 - 8 * g;
    unsigned zo = WS_ZT + 32 * c16 + 8 * g;
    unsigned l16 = lane * 16;
    float dv[3];
#pragma unroll
    for (int c = 0; c < 3; c++) dv[c] = __uint_as_float(rfl((unsigned)P.Dv[chs[c]] << 16));
    asm volatile("" : "+v"(vp[0]), "+v"(vp[1]), "+v"(vp[2]), "+v"(vq[0]), "+v"(vq[1]), "+v"(vq[2]), "+v"(zl[0]), "+v"(zl[1]), "+v"(zl[2]));
    asm volatile("" : "+v"(vwo[0]), "+v"(vwo[1]), "+v"(vwo[2]), "+v"(vwo[3]), "+v"(vwo[4]), "+v"(ka), "+v"(kx), "+v"(wl), "+v"(so), "+v"(la), "+v"(lb), "+v"(ld), "+v"(zo), "+v"(l16));
    __builtin_amdgcn_s_setprio(2);
    cnt_wait(C_QDONE, 4);
#pragma unroll
    for (int i = 0; i < 3; i++) {
      const int p = j + 4 * i;
      __builtin_amdgcn_s_sleep(PSL);
      if (p < 11) dma16(rk, vp[i] - 3840u, 0, L_KT + 1024 * p);
      __builtin_amdgcn_s_sleep(PSL);
      if (p < 10) dma16(rv, vp[i], 0, L_VT + 1024 * p);
    }
    if (j == 3) dma16(rsh, l16, 640 * hg, L_SH);
    auto hcopy = [&](int c) {
#pragma unroll
      for (int i = 0; i < 4; i++) { __builtin_amdgcn_s_sleep(HSL); dma16(rh, l16, chs[c] * 4096 + 2 * j + 1024 * i, L_HB + c * HBC + j * HCS + 32 + 1024 * i); }
    };
    hcopy(0);
    if (lane < 12) {
      const int c = lane >> 2;
      const u32x2 h4 = __builtin_amdgcn_raw_buffer_load_b64(rh, (c == 0 ? chs[0] : c == 1 ? chs[1] : chs[2]) * 4096, 0, 0);
      u64 hv = (((u64)h4[1] << 32) | h4[0]) << (16 * (3 - j)) << 16;
      if (tp != 3) hv = 0;
      *lp<u64>(L_HB + c * HBC + j * HCS + tp * 8) = hv;
    }
    WAITV(0);
    grp_bar(C_AUX, 4, lane);
    STAMP(1);
#pragma unroll
    for (int i = 0; i < 3; i++) {
      if (j + 4 * i < 10) {
        const u32x2 wt = *lp<u32x2>(wl + i * 256);
        const u32x2 e0 = trd(ka + i * 64 + 800), o0 = trd(ka + i * 64 + 640), e1 = trd(ka + i * 64 + 160), o1 = trd(ka + i * 64), xx = trd(kx + i * 64);
        const u32x2 v0 = trd(ka + i * 64 + (L_VT - L_KT) + 320), v1 = trd(ka + i * 64 + (L_VT - L_KT) - 320);
        SB();
        const unsigned pr[10] = {e0[0], o0[0], e0[1], o0[1], e1[0], o1[0], e1[1], o1[1], xx[0], xx[1]};
        float r[8];
#pragma unroll
        for (int e = 0; e < 8; e++) {
          const float t = dot2(pr[e], wt[0], dot2(pr[e + 2], wt[1], 0.f));
          const unsigned vw = e < 4 ? v0[e >> 1] : v1[(e - 4) >> 1];
          r[e] = t * ((e & 1) ? hi2f(vw) : lo2f(vw));
        }
        __builtin_amdgcn_raw_buffer_store_b128(u32x4{pk2(r[0], r[1]), pk2(r[2], r[3]), pk2(r[4], r[5]), pk2(r[6], r[7])}, rws, so, i * 131072, SC1);
      }
    }
    WAITV(0);
    if (cnt_arrive(C_F1, lane) == 3 && lane < 32) flag_store(P.ws, WS_F1 + (hg * 32 + lane) * 256 + rt * 8, tag << 16);
    STAMP(2);
    hcopy(1); hcopy(2);
    if (j == 0) { flag_poll<8>(P.ws, WS_F1 + (hg * 32 + rt) * 256 + col * 8, tag); prog_set(C_P1, 1); }
    else cnt_wait<6>(C_P1, 1);
    STAMP(3);
    {
      u32x4 t[3];
#pragma unroll
      for (int c = 0; c < 3; c++) t[c] = __builtin_amdgcn_raw_buffer_load_b128(rws, l16, WS_KVP + chs[c] * 4096 + 1024 * j, SC1);
      if (j < 3 && lane < 32) *lp<u32x4>(L_XR + j * XRC + 4096 + l16) = u32x4{0, 0, 0, 0};
#pragma unroll
      for (int c = 0; c < 3; c++) *lp<u32x4>(L_XR + c * XRC + 1024 * j + l16) = t[c];
      WAITV(0);
    }
    grp_bar(C_AUX, 8, lane);
    STAMP(4);
    auto wo_chunk = [&](int c) {
#pragma unroll
      for (int i = 0; i < 5; i++) dma16(rwo, vwo[i], c * 163840, L_WO + c * 20480 + j * 5120 + 1024 * i);
    };
    unsigned items = j == 0 ? (prod ? (7 | 3 << 5) : (7 | 3 << 5 | 17 << 10)) : j == 1 ? (15 | 11 << 5) : j == 3 ? (5 | 13 << 5)
                            : (prod ? (23 | 1 << 5 | 9 << 10) : (21 | 19 << 5 | 1 << 10 | 9 << 15));
    int wc = 0;
#pragma unroll 1
    for (; items; items >>= 5) {
      const int c = (items >> 3) & 3, t = items & 7;
      const float d = c == 0 ? dv[0] : c == 1 ? dv[1] : dv[2];
      const int ch = c == 0 ? chs[0] : c == 1 ? chs[1] : chs[2];
      const unsigned xo = c * XRC - 512 * t;
      const u32x2 k1 = *lp<u32x2>(ld + xo), k0 = *lp<u32x2>(ld + xo + 512);
      f32x4 acc1 = {d * hi2f(k1[1]), d * lo2f(k1[1]), d * hi2f(k1[0]), d * lo2f(k1[0])};
      f32x4 acc0 = {d * hi2f(k0[1]), d * lo2f(k0[1]), d * hi2f(k0[0]), d * lo2f(k0[0])};
      conv_pair(la + c * HBC, lb + xo, t, acc1, acc0, [&]() { if (wc < 8) { if (wc & 1) wo_chunk(((wc >> 1) + (b >> 6)) & 3); wc++; } });
      __builtin_amdgcn_raw_buffer_store_b64(u32x2{pk2(acc0[0], acc0[1]), pk2(acc0[2], acc0[3])}, rws, zo, ch * 4096 + 512 * t - 512, SC1);
      __builtin_amdgcn_raw_buffer_store_b64(u32x2{pk2(acc1[0], acc1[1]), pk2(acc1[2], acc1[3])}, rws, zo, ch * 4096 + 512 * t, SC1);
    }
    STAMP(5);
    WAITV(0);
    if (cnt_arrive(C_F3, lane) == 3 && lane < 32) flag_store(P.ws, WS_F3 + (hg * 32 + lane) * 256 + rt * 8, tag << 16);
    STAMP(6);
#pragma unroll
    for (int i = 0; i < 3; i++) qv[i] = __builtin_amdgcn_raw_buffer_load_b128(rq, vq[i], 160 * hg, 0);
    cnt_wait<6>(C_F3, 4);
    wo_chunk(4);
    if (j == 1) { __builtin_amdgcn_s_sleep(8); flag_poll<8>(P.ws, WS_F3 + (hg * 32 + rt) * 256 + col * 8, tag); prog_set(C_P3, 1); }
    else cnt_wait<6>(C_P3, 1);
    {
      u32x4 t[3];
      const unsigned zi = WS_ZT + (80 * hg + 8 * j + (lane >> 3)) * 4096 + rt * 128 + (lane & 7) * 16;
#pragma unroll
      for (int i = 0; i < 3; i++) if (j + 4 * i < 10) t[i] = __builtin_amdgcn_raw_buffer_load_b128(rws, zi, i * 131072, SC1);
#pragma unroll
      for (int i = 0; i < 3; i++) if (j + 4 * i < 10) *lp<u32x4>(L_ZB + (j + 4 * i) * 1024 + l16) = t[i];
    }
    STAMP(15);
    __builtin_amdgcn_s_setprio(0);
  } else {
    const int rs = wave & 1, kh = wave >> 1;
    if (TLBW && wave == 3) {
      __builtin_amdgcn_s_sleep(4);
      const unsigned ro = 64 * rt * 1280 + (lane & 1) * 63 * 1280 + 160 * hg;
      const unsigned co = (80 * hg + (lane & 1) * 79) * 4096;
      const int k = lane >> 1;
      const char* p = k == 0 ? (const char*)P.Ak + ro : k == 1 ? (const char*)P.Av + ro : k == 2 ? (const char*)P.Aq + ro : k == 3 ? (const char*)P.out + 2 * ro
                    : k == 4 ? (const char*)P.hk + (80 * hg + 5 * (rt >> 1) + 4 * (lane & 1)) * 4096 : k == 5 ? (const char*)P.wo + (lane & 1) * 639 * 1280
                    : k == 6 ? P.ws + WS_KVP + co : k == 7 ? P.ws + WS_ZT + co : k == 8 ? P.ws + WS_OX + rt * 81920 + (lane & 1) * 81000
                    : k == 9 ? ((lane & 1) ? (const char*)P.sh : (const char*)P.Dv) : P.ws + ((lane & 1) ? WS_IMG + 32 * REC + 4096 : 1024);
      if (k < 11) { unsigned t; asm volatile("global_load_dword %0, %1, off sc1\n\ts_waitcnt vmcnt(0)" : "=&v"(t) : "v"(p) : "memory"); }
    }
    unsigned xa[4];
#pragma unroll
    for (int i = 0; i < 4; i++) xa[i] = L_X + (4 * rs + (col >> 3)) * 1024 + (col & 7) * 128 + 16 * ((2 * i + h) ^ ((col >> 1) & 7));
    const unsigned wa = L_WQ + (8 * h + 2 * tq) * 160 + 80 * kh + wpc;
    f32x16 acc0 = {0}, acc1 = {0};
    bf16x8 xf[2][4], a0[2][4], a1[2][4];
    int hc = 2 * cc0;
    if (prod) cnt_wait(C_LA, 2);
    u32x2 lv = {0, 0};
#pragma unroll
    for (int t = 0; t < 4; t++) { o[t] = f32x16{0}; asm volatile("" : "+v"(o[t])); }
#pragma unroll
    for (int c = -1; c < 10; c++) {
      const int z = c & 1, n = z ^ 1;
      const unsigned w0 = wa + ((c + 1) % 5) * SLOT, xo = hc * 8192;
      if (c < 9) {
        if (__builtin_expect(rfl(lv[n]) < (unsigned)((c + 1) >> 1) + 1, 0)) cnt_wait<1>(C_LAND + n, ((c + 1) >> 1) + 1);
        asm volatile("" ::: "memory");
        if (c == -1) STAMP(1);
        if (c == 8) STAMP(15);
      }
#pragma unroll
      for (int k = 0; k < 4; k++) {
        SB();
        if (c >= 0) acc0 = MF32(a0[z][k], xf[z][k], acc0);
        SB();
        if (c < 9) { xf[n][k] = *lp<bf16x8>(xa[k] + xo); a0[n][k] = cat2(trd(w0 + k * 2560), trd(w0 + k * 2560 + 160)); }
        SB();
        if (c >= 0) acc1 = MF32(a1[z][k], xf[z][k], acc1);
        SB();
        if (c < 8 && k == 2) lv = *(volatile LAS u32x2*)(size_t)(L_MISC + 4 * C_LAND);
        if (c == 9 && k == 1) lv = *(volatile LAS u32x2*)(size_t)(L_MISC + 4 * C_KM);
        if (c < 9) a1[n][k] = cat2(trd(w0 + k * 2560 + 40), trd(w0 + k * 2560 + 200));
      }
      if (c < 4) prog_set(C_FREE + wave, c + 2);
      hc = hc == 9 ? 0 : hc + 1;
      SB();
    }
    STAMP(2);
    const bool early = rfl(lv[0]) != 0;
    if (early) loadrec<0>(rA, rB, rws, vk, vk2, vv, kh * 16 * REC);
    if (prod) cnt_wait(C_KVD, 2);
    u32x2 qn[4];
#pragma unroll
    for (int t = 0; t < 2; t++) {
      const f32x16& a = t ? acc1 : acc0;
      u32x4 w = {pk2(a[0], a[1]), pk2(a[2], a[3]), pk2(a[4], a[5]), pk2(a[6], a[7])};
      u32x2 w2 = {pk2(a[8], a[9]), pk2(a[10], a[11])};
      float n2 = xhalf_sum(dot2(w[0], w[0], dot2(w[1], w[1], dot2(w[2], w[2], dot2(w[3], w[3], 0.f)))));
      n2 = dot2(w2[0], w2[0], dot2(w2[1], w2[1], n2));
      if (h) w2 = u32x2{__float_as_uint(__builtin_amdgcn_sqrtf(n2)), 0u};
      q1[t] = asb(w); qn[t] = w2;
      *lp<u32x4>(L_QX + (wave * 2 + t) * 1536 + lane * 16) = w;
      *lp<u32x2>(L_QX + (wave * 2 + t) * 1536 + 1024 + lane * 8) = w2;
    }
    cnt_signal(C_MAIN);
    if (!early) { cnt_wait<1>(C_KM, 1); loadrec<0>(rA, rB, rws, vk, vk2, vv, kh * 16 * REC); }
    STAMP(4);
    cnt_wait<0>(C_MAIN, 4);
#pragma unroll
    for (int t = 0; t < 2; t++) {
      q1[2 + t] = *lp<bf16x8>(L_QX + ((wave ^ 2) * 2 + t) * 1536 + lane * 16);
      qn[2 + t] = *lp<u32x2>(L_QX + ((wave ^ 2) * 2 + t) * 1536 + 1024 + lane * 8);
    }
    const u32x4 mw = *lp<u32x4>(L_MISC + 4 * W_TAG);
    const float km = __uint_as_float(mw[3]) * -1.01f;
    tag = ((u64)rfl(mw[1]) << 32) | rfl(mw[0]);
    cnt_signal(C_QDONE);
#pragma unroll
    for (int t = 0; t < 4; t++) {
      if (h) qn[t][0] = pk2(__uint_as_float(qn[t][0]) * km, 0.f);
      q2[t] = as4(qn[t]);
    }
    loadrec<1>(rA, rB, rws, vk, vk2, vv, kh * 16 * REC);
    STAMP(3);
  }

  for (int attempt = 0; attempt < 2; attempt++) {
    if (wave < 4) {
      const int rs = wave & 1, kh = wave >> 1;
      const f32x16 zero = {0};
      if (attempt) {
        float mx[4];
#pragma unroll
        for (int t = 0; t < 4; t++) { mx[t] = -3.0e38f; if (h) q2[t] = s16x4{0, 0, 0, 0}; }
#pragma unroll 1
        for (int r = 0; r < 64; r++) {
          const unsigned so = (r >> 1) * REC;
          u32x4 k1 = __builtin_amdgcn_raw_buffer_load_b128(rws, vk + (r & 1) * 1024, so, SC1);
          u32x2 k2 = __builtin_amdgcn_raw_buffer_load_b64(rws, vk2 + (r & 1) * K2S, so, SC1);
#pragma unroll
          for (int t = 0; t < 4; t++) {
            f32x16 sv = MF32(asb(k1), q1[t], MF8(__builtin_bit_cast(s16x4, k2), q2[t], zero));
#pragma unroll
            for (int i = 0; i < 16; i++) mx[t] = fmaxf(mx[t], sv[i]);
          }
        }
#pragma unroll
        for (int t = 0; t < 4; t++) {
          const float m = -fmaxf(mx[t], __shfl_xor(mx[t], 32));
          const float m0 = hi2f(__float_as_uint(m)), m1 = hi2f(__float_as_uint(m - m0)), m2 = m - m0 - m1;
          if (h) q2[t] = __builtin_bit_cast(s16x4, u32x2{pk2(m0, m1), pk2(m2, 0.f)});
        }
        loadrec<0>(rA, rB, rws, vk, vk2, vv, kh * 16 * REC);
        loadrec<1>(rA, rB, rws, vk, vk2, vv, kh * 16 * REC);
#pragma unroll
        for (int t = 0; t < 4; t++) o[t] = zero;
      }
      unsigned soff = (kh * 16 + 1) * REC;
      f32x16 s[3];
      s[2] = zero; s[1] = zero;
      rB.v[3] = u32x4{0, 0, 0, 0};
      int idx = 0xdddd;
      asm volatile("" : "+v"(idx));
      __builtin_amdgcn_s_waitcnt(vmenc(12));
      s[0] = MF32(asb(rA.k1[0]), q1[0], MF8(as4(rA.k2[0]), q2[0], zero));
      STAMP(5);
#pragma unroll 1
      for (int it = 0; it < 8; it++) {
        const unsigned s2 = it == 7 ? 0u : soff + REC, s3 = it == 7 ? 0u : soff + 2 * REC;
        STAMPI(16 + 2 * it);
        dstep(rA, rB, s, o, q1, q2, idx, rws, vk, vk2, vv, soff, s2);
        STAMPI(17 + 2 * it);
        dstep(rB, rA, s, o, q1, q2, idx, rws, vk, vk2, vv, s2, s3);
        soff = s3;
      }
      o[3] = PV(asb(rB.v[3]), sub8<8>(s[2]), o[3]);
      STAMP(6);
      cnt_wait(C_F3, 4);
#pragma unroll
      for (int t = 0; t < 4; t++)
#pragma unroll
        for (int i = 0; i < 3; i++)
          *lp<f32x4>(L_OP + ((wave * 4 + ((2 * kh + t) & 3)) * 3 + i) * 1024 + lane * 16) = f32x4{o[t][4 * i], o[t][4 * i + 1], o[t][4 * i + 2], o[t][4 * i + 3]};
    }
    WAITV(0);
    WAITL();
    __builtin_amdgcn_s_barrier();
    STAMP(9);
    {
      const int rs = wave & 1, hl = wave >> 1;
      const unsigned ox = WS_OX + ((rt * 8 + hg) * 2 + rs) * 5120;
      f32x4 r[3];
#pragma unroll
      for (int i = 0; i < 3; i++)
        r[i] = *lp<f32x4>(L_OP + ((rs * 4 + hl) * 3 + i) * 1024 + lane * 16) + *lp<f32x4>(L_OP + (((rs + 2) * 4 + hl) * 3 + i) * 1024 + lane * 16);
      const float inv = __builtin_amdgcn_rcpf(r[2][2]);
      __builtin_amdgcn_raw_buffer_store_b128(u32x4{pk2(r[0][0] * inv, r[0][1] * inv), pk2(r[0][2] * inv, r[0][3] * inv), pk2(r[1][0] * inv, r[1][1] * inv), pk2(r[1][2] * inv, r[1][3] * inv)},
                                             rws, ox + hl * 1024 + lane * 16, 0, SC1);
      if (!(r[2][2] >= 9.0e-13f)) *lp<unsigned>(L_MISC + 4 * C_BAD) = 1;
      if (hl == 3) {
        u32x4 e;
#pragma unroll
        for (int t = 0; t < 4; t++) {
          f32x4 y = *lp<f32x4>(L_OP + ((rs * 4 + t) * 3 + 2) * 1024 + lane * 16) + *lp<f32x4>(L_OP + (((rs + 2) * 4 + t) * 3 + 2) * 1024 + lane * 16);
          const float iv = __builtin_amdgcn_rcpf(y[2]);
          e[t] = pk2(y[0] * iv, y[1] * iv);
        }
        __builtin_amdgcn_raw_buffer_store_b128(e, rws, ox + 4096 + lane * 16, 0, SC1);
      }
    }
    WAITV(0);
    WAITL();
    __builtin_amdgcn_s_barrier();
    if (rfl(*(volatile LAS unsigned*)(size_t)(L_MISC + 4 * C_BAD)) == 0) break;
  }
  if (tid < 8) flag_store(P.ws, WS_F4 + (rt * 8 + tid) * 128 + hg * 8, tag << 16);
  STAMP(10);
  const rsrc_t rout = mkrsrc(P.out, S * D * 4);
  if (wave >= 4) {
#pragma unroll
    for (int i = 0; i < 3; i++) {
      if (j + 4 * i < 10) {
        const u32x2 z0 = trd(zl[i]), z1 = trd(zl[i] + 512);
        u32x4 r;
        r[0] = pk2(lo2f(qv[i][0]) * lo2f(z0[0]), hi2f(qv[i][0]) * hi2f(z0[0]));
        r[1] = pk2(lo2f(qv[i][1]) * lo2f(z0[1]), hi2f(qv[i][1]) * hi2f(z0[1]));
        r[2] = pk2(lo2f(qv[i][2]) * lo2f(z1[0]), hi2f(qv[i][2]) * hi2f(z1[0]));
        r[3] = pk2(lo2f(qv[i][3]) * lo2f(z1[1]), hi2f(qv[i][3]) * hi2f(z1[1]));
        __builtin_amdgcn_raw_buffer_store_b128(r, rout, 2 * vq[i] - (vq[i] & 255), 160 * hg, 0);
      }
    }
  }

  {
    const int r16 = wave & 3, kh = wave >> 2;
    const unsigned ob = WS_OX + ((rt * 8 + 4 * kh) * 2 + (r16 >> 1)) * 5120 + ((g >> 1) * 32 + (r16 & 1) * 16 + c16) * 16;
    const unsigned wm = L_WO + (320 * kh + 20 * (g & 1) + 8 * (g >> 1) + tq) * 160 + tp * 8;
    const unsigned wr = L_WO + (320 * kh + 80 * (g & 1) + 20 * (tq >> 1) + 16 + 2 * (g >> 1) + (tq & 1)) * 160 + tp * 8;
    auto wfrag = [&](int i, int m) {
      return i < 8 ? cat2(trd(wm + i * 6400 + m * 32), trd(wm + i * 6400 + m * 32 + 640))
                   : cat2(trd(wr + (i - 8) * 25600 + m * 32), trd(wr + (i - 8) * 25600 + m * 32 + 6400));
    };
    constexpr int NPRE = 6;
    bf16x8 wf[10][5];
#pragma unroll
    for (int i = 0; i < NPRE; i++)
#pragma unroll
      for (int m = 0; m < 5; m++) wf[i][m] = wfrag(i, m);
    SB();
    flag_poll<1>(P.ws, WS_F4 + (rt * 8 + hg) * 128 + (lane & 7) * 8, tag);
    STAMP(11);
    u32x4 of[10];
#pragma unroll
    for (int i = 0; i < 8; i++) { of[i] = __builtin_amdgcn_raw_buffer_load_b128(rws, ob + (g & 1) * 1024, (i >> 1) * 10240 + (i & 1) * 2048, SC1); SB(); }
#pragma unroll
    for (int i = 0; i < 2; i++) { of[8 + i] = __builtin_amdgcn_raw_buffer_load_b128(rws, ob + (g & 1) * 10240 + 4096, i * 20480, SC1); SB(); }
    if (b == 0 && tid == 0) *(u64*)(P.ws + WS_EPOCH) = tag;
    f32x4 acc[5];
#pragma unroll
    for (int m = 0; m < 5; m++) acc[m] = f32x4{0, 0, 0, 0};
#pragma unroll
    for (int i = 0; i < 10; i++) {
#pragma unroll
      for (int m = 0; m < 5; m++) {
        SB();
        acc[m] = MF16(wf[i][m], asb(of[i]), acc[m]);
        SB();
        if (i + NPRE < 10) wf[i + NPRE][m] = wfrag(i + NPRE, m);
      }
    }
    STAMP(12);
    if (kh) {
#pragma unroll
      for (int m = 0; m < 5; m++) *lp<f32x4>(L_RED + (r16 * 5 + m) * 1024 + lane * 16) = acc[m];
    }
    WAITL();
    __builtin_amdgcn_s_barrier();
    if (!kh) {
#pragma unroll
      for (int m = 0; m < 5; m++) {
        f32x4 y = acc[m] + *lp<f32x4>(L_RED + (r16 * 5 + m) * 1024 + lane * 16);
        __builtin_amdgcn_raw_buffer_store_b64(u32x2{pk2(y[0], y[1]), pk2(y[2], y[3])}, rout, (64 * rt + 16 * r16 + c16) * 2560 + 1280 + 160 * hg + 32 * m + 8 * g, 0, 0);
      }
    }
    STAMP(13);
    STAMP_OUT();
  }
}

extern "C" void kernel_launch(void* const* d_in, const int* in_sizes, int n_in,
                              void* d_out, int out_size, void* d_ws, size_t ws_size,
                              hipStream_t stream) {
  Params P;
  P.Aq = (const u16*)d_in[0]; P.Ak = (const u16*)d_in[1]; P.Av = (const u16*)d_in[2];
  P.sh = (const u16*)d_in[3]; P.hk = (const u16*)d_in[4]; P.Dv = (const u16*)d_in[5];
  P.x = (const u16*)d_in[6]; P.wq = (const u16*)d_in[7]; P.wk = (const u16*)d_in[8];
  P.wv = (const u16*)d_in[9]; P.wo = (const u16*)d_in[10];
  P.out = (u16*)d_out; P.ws = (char*)d_ws;
  (void)hipFuncSetAttribute((const void*)fused_kernel, hipFuncAttributeMaxDynamicSharedMemorySize, LDS_BYTES);
  fused_kernel<<<256, 512, LDS_BYTES, stream>>>(P);
}
```

```cpp
#define HURRY 3
#include <hip/hip_runtime.h>
#include <hip/hip_bf16.h>

typedef unsigned short u16;
typedef unsigned long long u64;
typedef __attribute__((ext_vector_type(16))) float f32x16;
typedef __attribute__((ext_vector_type(8))) float f32x8;
typedef __attribute__((ext_vector_type(4))) float f32x4;
typedef __attribute__((ext_vector_type(2))) float f32x2;
typedef __attribute__((ext_vector_type(16))) __bf16 bf16x16;
typedef __attribute__((ext_vector_type(8))) __bf16 bf16x8;
typedef __attribute__((ext_vector_type(2))) __bf16 bf16x2;
typedef __attribute__((ext_vector_type(4))) short s16x4;
typedef __attribute__((ext_vector_type(4))) unsigned u32x4;
typedef __attribute__((ext_vector_type(2))) unsigned u32x2;
typedef __amdgpu_buffer_rsrc_t rsrc_t;
#define LAS __attribute__((address_space(3)))
#define GAS __attribute__((address_space(1)))
#define DI __device__ __forceinline__
#define SB() __builtin_amdgcn_sched_barrier(0)

constexpr int S = 2048, D = 640;
constexpr float KSCALE = 0.22360679775f * 1.44269504089f;

constexpr unsigned WS_EPOCH = 0;
constexpr unsigned WS_F1 = 65536;
constexpr unsigned WS_F2 = 131072;
constexpr unsigned WS_F3 = 196608;
constexpr unsigned WS_F4 = 262144;
constexpr unsigned WS_KVP = 1u << 20;
constexpr unsigned WS_IMG = 4u << 20;
constexpr unsigned WS_ZT = 5u << 20;
constexpr unsigned WS_OX = 8u << 20;
constexpr unsigned WS_BYTES = 12u << 20;
constexpr unsigned REC = 5376, K2OFF = 2048, K2S = 320, VOFF = 2688, VFR = 672;

constexpr unsigned L_X = 0, L_WKV = 81920, L_WQ = 107520, SLOT = 10240, L_QX = L_WQ + 2 * SLOT, L_KVR = 158720;
constexpr unsigned QRT = 8 * REC, L_SB = 0, L_SA = 81920;
constexpr unsigned HCS = 4160, L_H0 = 43008, L_H1 = L_H0 + 4 * HCS, L_H2 = 124928, L_SH = 76288, L_JUNK = 77312;
constexpr unsigned L_KT = 141568, L_VT = L_KT + 11264;
constexpr unsigned L_XR = L_KT, XRC = 4608;
constexpr unsigned L_WO = 43008, L_ZB = 145408, L_OP = 0, L_RED = 0;
constexpr unsigned L_MISC = 163328;
constexpr unsigned LDS_BYTES = 163840;
enum { C_MAIN = 0, C_AUX = 1, C_F1 = 2, C_F4 = 3, C_F3 = 4, C_KVD = 5, C_QDONE = 6, C_KVP = 7, C_LI = 8, C_LA = 9, C_LAND = 10, W_TAG = 12, C_KM = 14, W_KM = 15,
       C_FREE = 16, C_MP = 20, C_OPW = 24, C_P1 = 28, C_P3 = 29, C_AI = 30, C_WO = 31, C_KVS = 32 };
#define PKFMA 0
#define AF 0
#define PWAVE 0
#define PSL 1
#define FLATE 8
#define NPOLL 4

constexpr unsigned CODE_SPAN = 37888;
constexpr int SC1 = 16;
constexpr unsigned OOB = 0x80000000u;
constexpr unsigned SPIN_LDS = 1u << 20, SPIN_GLB = 1u << 17;

struct Params { const u16 *Aq, *Ak, *Av, *sh, *hk, *Dv, *x, *wq, *wk, *wv, *wo; u16* out; char* ws; };

DI unsigned umin(unsigned a, unsigned b) { return a < b ? a : b; }
DI unsigned umax(unsigned a, unsigned b) { return a > b ? a : b; }
DI float lo2f(unsigned v) { return __uint_as_float(v << 16); }
DI float hi2f(unsigned v) { return __uint_as_float(v & 0xffff0000u); }
DI unsigned pk2(float a, float b) { bf16x2 t = {(__bf16)a, (__bf16)b}; return __builtin_bit_cast(unsigned, t); }
DI float dot2(unsigned a, unsigned b, float c) { return __builtin_amdgcn_fdot2_f32_bf16(__builtin_bit_cast(bf16x2, a), __builtin_bit_cast(bf16x2, b), c, false); }
DI float smul(float s, float v) { float r; asm("v_mul_f32 %0, %1, %2" : "=v"(r) : "s"(s), "v"(v)); return r; }
DI float vmul(float a, float b) { float r; asm("v_mul_f32 %0, %1, %2" : "=v"(r) : "v"(a), "v"(b)); return r; }
DI rsrc_t mkrsrc(const void* p, unsigned n) { return __builtin_amdgcn_make_buffer_rsrc((void*)p, 0, n, 0x00020000); }
DI void dma16(rsrc_t rs, unsigned voff, unsigned soff, unsigned lds) {
  asm volatile("s_mov_b32 m0, %0\n\ts_nop 0\n\tbuffer_load_dwordx4 %1, %2, %3 offen lds" :: "s"(lds), "v"(voff), "s"(rs), "s"(soff) : "memory");
}
DI void dma16c(rsrc_t rs, unsigned voff, unsigned soff, unsigned lds) {
  asm volatile("s_mov_b32 m0, %0\n\ts_nop 0\n\tbuffer_load_dwordx4 %1, %2, %3 offen sc1 lds" :: "s"(lds), "v"(voff), "s"(rs), "s"(soff) : "memory");
}
DI void dma4(rsrc_t rs, unsigned voff, unsigned soff, unsigned lds) {
  asm volatile("s_mov_b32 m0, %0\n\ts_nop 0\n\tbuffer_load_dword %1, %2, %3 offen lds" :: "s"(lds), "v"(voff), "s"(rs), "s"(soff) : "memory");
}
template <int AUX> DI void st128(u32x4 v, rsrc_t rs, unsigned voff, unsigned soff) {
  __builtin_amdgcn_raw_buffer_store_b128(v, rs, voff, soff, AUX);
  asm volatile("s_nop 1" :: "v"(v) : "memory");
}
#define WAITV(n) asm volatile("s_waitcnt vmcnt(" #n ")" ::: "memory")
#define WAITL() asm volatile("s_waitcnt lgkmcnt(0)" ::: "memory")
template <typename T> DI LAS T* lp(unsigned a) { return (LAS T*)(size_t)a; }
DI u32x2 trd(unsigned a) { return __builtin_bit_cast(u32x2, __builtin_amdgcn_ds_read_tr16_b64_v4i16(lp<s16x4>(a))); }
DI bf16x8 cat2(u32x2 a, u32x2 b) { return __builtin_bit_cast(bf16x8, __builtin_shufflevector(a, b, 0, 1, 2, 3)); }
DI bf16x8 asb(u32x4 v) { return __builtin_bit_cast(bf16x8, v); }
DI unsigned rfl(unsigned v) { return __builtin_amdgcn_readfirstlane(v); }

DI unsigned cnt_arrive(int c, int lane) {
  unsigned r; u64 save;
  asm volatile("s_waitcnt lgkmcnt(0)\n\ts_mov_b64 %1, exec\n\ts_mov_b64 exec, 1\n\tds_add_rtn_u32 %0, %2, %3\n\ts_mov_b64 exec, %1\n\ts_waitcnt lgkmcnt(0)"
               : "=&v"(r), "=&s"(save) : "v"(L_MISC + 4 * c), "v"(1u) : "memory");
  return rfl(r);
}
DI void cnt_signal(int c) {
  u64 save;
  asm volatile("s_waitcnt lgkmcnt(0)\n\ts_mov_b64 %0, exec\n\ts_mov_b64 exec, 1\n\tds_add_u32 %1, %2\n\ts_mov_b64 exec, %0"
               : "=&s"(save) : "v"(L_MISC + 4 * c), "v"(1u) : "memory");
}
template <int SLP = 2> DI void cnt_wait(int c, unsigned target) {
#pragma nounroll
  for (unsigned spins = 0; spins < SPIN_LDS; ++spins) {
    unsigned v = __hip_atomic_load(lp<unsigned>(L_MISC + 4 * c), __ATOMIC_RELAXED, __HIP_MEMORY_SCOPE_WORKGROUP);
    if (rfl(v) >= target) break;
    __builtin_amdgcn_s_sleep(SLP);
  }
  asm volatile("" ::: "memory");
}
DI void cnt_wait2(int c, unsigned target) {
#pragma nounroll
  for (unsigned spins = 0; spins < SPIN_LDS; ++spins) {
    u32x2 v = *(volatile LAS u32x2*)(size_t)(L_MISC + 4 * c);
    if (rfl(umin(v[0], v[1])) >= target) break;
    __builtin_amdgcn_s_sleep(1);
  }
  asm volatile("" ::: "memory");
}
template <int SLP = 1> DI void cnt_wait4(int c, unsigned target) {
#pragma nounroll
  for (unsigned spins = 0; spins < SPIN_LDS; ++spins) {
    u32x4 v = *(volatile LAS u32x4*)(size_t)(L_MISC + 4 * c);
    if (rfl(umin(umin(v[0], v[1]), umin(v[2], v[3]))) >= target) break;
    __builtin_amdgcn_s_sleep(SLP);
  }
  asm volatile("" ::: "memory");
}
DI void prog_set(int c, unsigned v) { asm volatile("" ::: "memory"); *(volatile LAS unsigned*)(size_t)(L_MISC + 4 * c) = v; asm volatile("" ::: "memory"); }
DI void grp_bar(int c, unsigned target, int lane) { if (cnt_arrive(c, lane) + 1 < target) cnt_wait<4>(c, target); asm volatile("" ::: "memory"); }

struct Tag { unsigned lo, hi; };
DI void flag_store(rsrc_t rs, unsigned voff, unsigned soff, Tag t, unsigned v16 = 0) { __builtin_amdgcn_raw_buffer_store_b64(u32x2{t.lo | v16, t.hi}, rs, voff, soff, SC1); }
DI bool flag_ok(u32x2 v, Tag t) { return (((v[0] ^ t.lo) >> 16) | (v[1] ^ t.hi)) == 0; }
template <int SLP> DI void flag_poll(rsrc_t rs, unsigned voff, unsigned soff, Tag t) {
#pragma nounroll
  for (unsigned spins = 0; spins < SPIN_GLB; ++spins) {
    asm volatile("" ::: "memory");
    const u32x2 v = __builtin_amdgcn_raw_buffer_load_b64(rs, voff, soff, SC1);
    if (__all(flag_ok(v, t))) break;
    __builtin_amdgcn_s_sleep(SLP);
  }
  asm volatile("" ::: "memory");
}
DI unsigned flag_poll2(rsrc_t rs, unsigned voff, unsigned soff, Tag t) {
  u32x2 v = {0, 0};
#pragma nounroll
  for (unsigned spins = 0; spins < SPIN_GLB; ++spins) {
    asm volatile("" ::: "memory");
    v = __builtin_amdgcn_raw_buffer_load_b64(rs, voff, soff, SC1);
    const u32x2 w = __builtin_amdgcn_raw_buffer_load_b64(rs, voff, soff + 512, SC1);
    if (__all(flag_ok(v, t) && flag_ok(w, t))) break;
    __builtin_amdgcn_s_sleep(2);
  }
  asm volatile("" ::: "memory");
  return v[0];
}

#define MF32(a, b, c) __builtin_amdgcn_mfma_f32_32x32x16_bf16(a, b, c, 0, 0, 0)
#define MF8(a, b, c) __builtin_amdgcn_mfma_f32_32x32x8bf16_1k(a, b, c, 0, 0, 0)
#define MF16(a, b, c) __builtin_amdgcn_mfma_f32_16x16x32_bf16(a, b, c, 0, 0, 0)
#define MF16A(c, a, b) asm volatile("v_mfma_f32_16x16x32_bf16 %0, %1, %2, %0" : "+v"(c) : "v"(a), "v"(b))
#define PV(v, p, o) __builtin_amdgcn_smfmac_f32_32x32x32_bf16(v, p, o, idx, 0, 0)

#define STAMP(i)
#define STAMPI(i)
#define STAMP_DECL
#define STAMP_OUT()

template <int O> DI bf16x16 sub8(f32x16 v) {
  f32x8 t = __builtin_shufflevector(v, v, O, O + 1, O + 2, O + 3, O + 4, O + 5, O + 6, O + 7);
  return __builtin_bit_cast(bf16x16, t);
}
template <int A, int B> DI void exps(f32x16& s) {
#pragma unroll
  for (int i = A; i < B; i++) s[i] = __builtin_amdgcn_exp2f(s[i]);
}
DI s16x4 as4(u32x2 v) { return __builtin_bit_cast(s16x4, v); }
struct Rec { u32x4 k1[2]; u32x2 k2[2]; u32x4 v[4]; };
constexpr int vmenc(int n) { return (n & 15) | ((n >> 4) << 14) | (7 << 4) | (15 << 8); }

template <typename F3, typename F4, typename F6>
DI void tile(f32x16& sc, f32x16& sprev, f32x16& oprev, f32x16& ocur, bf16x8 vprev_b, bf16x8 vcur_a,
             bf16x8 kn1, s16x4 kn2, bf16x8 qn1, s16x4 qn2, f32x16& sn, int idx, F3 f3, F4 f4, F6 f6) {
  const f32x16 zero = {0};
  SB(); f32x16 t = MF8(kn2, qn2, zero);
  SB(); exps<0, 4>(sc);
  SB(); t = MF32(kn1, qn1, t);
  SB(); f3();
  SB(); exps<4, 8>(sc);
  SB(); oprev = PV(vprev_b, sub8<8>(sprev), oprev);
  SB(); f4();
  SB(); exps<8, 12>(sc);
  SB(); ocur = PV(vcur_a, sub8<0>(sc), ocur);
  SB(); f6();
  SB(); exps<12, 16>(sc);
  SB(); sn = t;
}
constexpr int lgenc(int n) { return 15 | (3 << 14) | (7 << 4) | (n << 8); }
template <typename H1, typename H3>
DI void lstep(Rec& c, f32x16 (&s)[3], f32x16 (&o)[2], const bf16x8 (&q1)[2], const s16x4 (&q2)[2], int idx,
              unsigned cv, unsigned nk, unsigned nk2, unsigned nv, H1 h1, H3 h3) {
  auto W = []() { __builtin_amdgcn_s_waitcnt(lgenc(1)); };
#define L_(dst, T, a) [&]() { dst = *lp<T>(a); }
  tile(s[0], s[2], o[1], o[0], asb(c.v[3]), asb(c.v[0]), asb(c.k1[0]), as4(c.k2[0]), q1[1], q2[1], s[1], idx, W, L_(c.v[3], u32x4, cv + 3 * VFR), L_(c.k1[0], u32x4, nk));
  tile(s[1], s[0], o[0], o[1], asb(c.v[1]), asb(c.v[0]), asb(c.k1[1]), as4(c.k2[1]), q1[0], q2[0], s[2], idx, h1, L_(c.k2[0], u32x2, nk2), L_(c.v[0], u32x4, nv));
  tile(s[2], s[1], o[1], o[0], asb(c.v[1]), asb(c.v[2]), asb(c.k1[1]), as4(c.k2[1]), q1[1], q2[1], s[0], idx, W, L_(c.v[1], u32x4, nv + VFR), L_(c.k1[1], u32x4, nk + 1024));
  tile(s[0], s[2], o[0], o[1], asb(c.v[3]), asb(c.v[2]), asb(c.k1[0]), as4(c.k2[0]), q1[0], q2[0], s[1], idx, h3, L_(c.k2[1], u32x2, nk2 + K2S), L_(c.v[2], u32x4, nv + 2 * VFR));
  f32x16 a = s[1], bb = s[0]; s[0] = a; s[2] = bb;
#undef L_
}

DI u32x4 lda(unsigned a) { u32x2 x = *(volatile LAS u32x2*)(size_t)a, y = *(volatile LAS u32x2*)(size_t)(a + 8); return __builtin_shufflevector(x, y, 0, 1, 2, 3); }

template <bool SWAP>
DI void kv_half(f32x16& acc, const unsigned (&xa)[4], unsigned wa) {
  bf16x8 xf[2][4], wf[2][4];
#pragma unroll
  for (int i = -1; i < 5; i++) {
    const int l = i + 1;
    if (l < 5) {
      if (l == 0) WAITV(28); else if (l == 1) WAITV(21); else if (l == 2) WAITV(14); else if (l == 3) WAITV(7); else { WAITV(0); cnt_signal(C_LA); }
#pragma unroll
      for (int k = 0; k < 4; k++) {
        xf[l & 1][k] = *lp<bf16x8>(xa[k] + l * 8192);
        wf[l & 1][k] = cat2(trd(wa + l * 2560 + k * 640), trd(wa + l * 2560 + k * 640 + 160));
      }
    }
    SB();
    if (i >= 0) {
#pragma unroll
      for (int k = 0; k < 4; k++) acc = SWAP ? MF32(xf[i & 1][k], wf[i & 1][k], acc) : MF32(wf[i & 1][k], xf[i & 1][k], acc);
    }
    SB();
  }
}
DI float xhalf_sum(float v) {
  auto r = __builtin_amdgcn_permlane32_swap(__float_as_uint(v), __float_as_uint(v), false, false);
  return __uint_as_float(r[0]) + __uint_as_float(r[1]);
}

__global__ __launch_bounds__(512, 2) void fused_kernel(Params P) {
  extern __shared__ __attribute__((aligned(16))) char smem_raw[];
  const int tid = threadIdx.x, lane = tid & 63;
  const int wave = rfl(tid >> 6);
  const int b = blockIdx.x;
  const int rt = 4 * (b & 7) + (b >> 6), hg = (b >> 3) & 7;
  const bool prod = !((rt + hg) & 1), isV = hg >= 4;
  const int prs = (hg >> 1) & 1;
  const int col = lane & 31, h = lane >> 5, g = lane >> 4, c16 = lane & 15, tq = (lane & 15) >> 2, tp = lane & 3;
  const rsrc_t rws = mkrsrc(P.ws, WS_BYTES);
  asm volatile("" :: "v"(smem_raw));
  u64 pc; asm volatile("s_getpc_b64 %0" : "=s"(pc));
  STAMP_DECL;
  STAMP(0);
  if (tid < 64) *lp<unsigned>(L_MISC + 4 * tid) = tid == C_KVS + 5 ? 4 : 0;
  asm volatile("" :: "s"(P.Ak), "s"(P.x), "s"(P.wq), "s"(P.wk), "s"(P.ws));
  WAITL();
  __builtin_amdgcn_s_barrier();
  Tag tag = {0, 0};
  const int j = wave - 4;
  const int cc0 = (hg + (b >> 6)) % 5;
  const unsigned wpc = (g & 1) ? 32 : ((tp & 1) * 2 + (tp >> 1)) * 8;
  const rsrc_t rout = mkrsrc(P.out, S * D * 4);
  unsigned vq[3], zl[3]; u32x4 qv[3];
  const int r16 = wave & 3, okh = wave >> 2;
  unsigned wm = L_WO + (320 * okh + 20 * (g & 1) + 8 * (g >> 1) + tq) * 160 + tp * 8;
  unsigned wr = L_WO + (320 * okh + 80 * (g & 1) + 20 * (tq >> 1) + 16 + 2 * (g >> 1) + (tq & 1)) * 160 + tp * 8;

  if (wave >= 4) {
    const int cb = 80 * hg + 5 * (rt >> 1);
    const int cb0 = cb + 3 * (rt & 1);
    auto chn = [=](int c) { return c == 2 ? cb + 2 : cb0 + c; };
    auto hbase = [](int c) { return c == 2 ? L_H2 : L_H0 + c * 4 * HCS; };
    unsigned dw[3];
    asm volatile("s_load_dword %0, %3, %4\n\ts_load_dword %1, %3, %5\n\ts_load_dword %2, %3, %6"
                 : "=&s"(dw[0]), "=&s"(dw[1]), "=&s"(dw[2]) : "s"(P.Dv), "s"((chn(0) >> 1) * 4), "s"((chn(1) >> 1) * 4), "s"((chn(2) >> 1) * 4) : "memory");
    const rsrc_t rx = mkrsrc(P.x, S * D * 2);
    unsigned vx[2];
#pragma unroll
    for (int i = 0; i < 2; i++) { int r7 = lane >> 3, pos = lane & 7; vx[i] = (64 * rt + 8 * i + r7) * 1280 + 16 * (pos ^ (4 * i + (r7 >> 1))); }
    auto xhalf = [&](int r, int hc) {
      const unsigned sx = 128 * hc + r * 40960, lx = L_X + hc * 8192 + r * 4096;
      dma16(rx, vx[0], sx, lx); dma16(rx, vx[1], sx, lx + 1024);
      dma16(rx, vx[0], sx + 20480, lx + 2048); dma16(rx, vx[1], sx + 20480, lx + 3072);
    };
    if (j < 2) {
      const rsrc_t rwq = mkrsrc(P.wq, D * D * 2);
      unsigned vw[5];
#pragma unroll
      for (int i = 0; i < 5; i++) { unsigned u = i * 64 + lane, pr = (u * 6554u) >> 16, ch = u - pr * 10, kk = (pr & ~7u) + ((pr & 1) << 2) + ((pr >> 1) & 3); vw[i] = kk * 1280 + 160 * hg + 16 * ch; }
      int hc = 2 * cc0 + j;
      if (prod) cnt_wait<1>(C_AI, 2);
      STAMP(7);
#pragma unroll 1
      for (int c = j; c < 10; c += 2) {
        if (c >= 5) cnt_wait4(C_FREE, c - 4);
        const unsigned slot = L_WQ + (c >= 5 ? c - 5 : c) * SLOT;
        if (!prod) xhalf(prs, hc);
        xhalf(prs ^ 1, hc);
#pragma unroll
        for (int i = 0; i < 5; i++) dma16(rwq, vw[i], 81920 * hc, slot + 1024 * i);
#pragma unroll
        for (int i = 0; i < 5; i++) dma16(rwq, vw[i], 81920 * hc + 40960, slot + 5120 + 1024 * i);
        if (c >= 2) {
          if (prod) WAITV(14); else WAITV(18);
          prog_set(C_LAND + j, c >> 1);
        }
        hc = hc >= 8 ? hc - 8 : hc + 2;
      }
      WAITV(0);
      prog_set(C_LAND + j, 5);
      STAMP(8);
    } else {
      __builtin_amdgcn_s_setprio(3);
      const int jj = j - 2;
      if (prod) {
        const rsrc_t rwkv = mkrsrc(isV ? P.wv : P.wk, D * 40);
#pragma unroll 1
        for (int i = 5 * jj; i < 5 * jj + 5; i++) {
          xhalf(prs, i);
          dma16(rwkv, 16 * lane, 2560 * i, L_WKV + 2560 * i);
          dma16(rwkv, 16 * lane, 2560 * i + 1024, L_WKV + 2560 * i + 1024);
          if (lane < 32) dma16(rwkv, 16 * lane, 2560 * i + 2048, L_WKV + 2560 * i + 2048);
        }
        cnt_signal(C_AI);
      } else if (jj == 0) {
        const unsigned o = ((b >> 4) * 20 + lane) * 128;
        if (lane < 20 && o < CODE_SPAN) { unsigned t; asm volatile("global_load_dword %0, %1, %2\n\ts_waitcnt vmcnt(0)" : "=&v"(t) : "v"(o), "s"(pc & ~127ull) : "memory"); }
      }
      u64 epoch;
      asm volatile("s_load_dwordx2 %0, %1, 0x0\n\ts_waitcnt lgkmcnt(0)" : "=s"(epoch) : "s"(P.ws) : "memory");
      epoch += 1;
      tag.lo = (unsigned)epoch << 16; tag.hi = (unsigned)(epoch >> 16);
      if (prod) {
        const int xr = isV ? (col & 19) | ((col & 4) << 1) | ((col & 8) >> 1) : col;
        unsigned xa[4];
#pragma unroll
        for (int i = 0; i < 4; i++) xa[i] = L_X + jj * 40960 + (4 * prs + (xr >> 3)) * 1024 + (xr & 7) * 128 + 16 * ((2 * i + h) ^ ((xr >> 1) & 7));
        const unsigned wa = L_WKV + jj * 12800 + (8 * h + tq) * 40 + wpc;
        f32x16 acc = {0};
        if (isV) kv_half<true>(acc, xa, wa); else kv_half<false>(acc, xa, wa);
        cnt_signal(C_KVD);
        if (!jj) STAMP(7);
        if (jj) {
#pragma unroll
          for (int i = 0; i < 4; i++) *lp<f32x4>(L_KVR + i * 1024 + lane * 16) = f32x4{acc[4 * i], acc[4 * i + 1], acc[4 * i + 2], acc[4 * i + 3]};
          cnt_signal(C_KVP);
        } else {
          cnt_wait<0>(C_KVP, 1);
#pragma unroll
          for (int i = 0; i < 4; i++) { f32x4 y = *lp<f32x4>(L_KVR + i * 1024 + lane * 16); acc[4 * i] += y[0]; acc[4 * i + 1] += y[1]; acc[4 * i + 2] += y[2]; acc[4 * i + 3] += y[3]; }
          const unsigned rec = WS_IMG + rt * REC;
          unsigned kb = 0;
          if (!isV) {
            u32x4 w = {pk2(acc[0] * KSCALE, acc[1] * KSCALE), pk2(acc[2] * KSCALE, acc[3] * KSCALE), pk2(acc[4] * KSCALE, acc[5] * KSCALE), pk2(acc[6] * KSCALE, acc[7] * KSCALE)};
            u32x2 w2 = {pk2(acc[8] * KSCALE, acc[9] * KSCALE), pk2(acc[10] * KSCALE, acc[11] * KSCALE)};
            st128<SC1>(w, rws, rec + prs * 1024 + lane * 16, 0);
            __builtin_amdgcn_raw_buffer_store_b64(h ? u32x2{0x3f803f80u, 0x00003f80u} : w2, rws, lane <= 32 ? rec + K2OFF + prs * K2S + lane * 8 : OOB, 0, SC1);
            float n2 = xhalf_sum(dot2(w[0], w[0], dot2(w[1], w[1], dot2(w[2], w[2], dot2(w[3], w[3], 0.f)))));
            n2 = dot2(w2[0], w2[0], dot2(w2[1], w2[1], n2));
#pragma unroll
            for (int o = 16; o; o >>= 1) n2 = __uint_as_float(umax(__float_as_uint(n2), __float_as_uint(__shfl_xor(n2, o))));
            kb = (__float_as_uint(__builtin_amdgcn_sqrtf(n2)) + 0xffffu) >> 16;
          } else {
            const unsigned cv = col == 20 ? 0x3f803f80u : 0u;
            const unsigned uo = col < 20 ? (h * 20 + col) * 16 : (col < 22 && !h) ? (20 + col) * 16 : OOB;
#pragma unroll
            for (int f = 0; f < 2; f++) {
              u32x4 w = {pk2(acc[8 * f], acc[8 * f + 1]), pk2(acc[8 * f + 2], acc[8 * f + 3]), pk2(acc[8 * f + 4], acc[8 * f + 5]), pk2(acc[8 * f + 6], acc[8 * f + 7])};
              if (col >= 20) w = u32x4{cv, cv, cv, cv};
              st128<SC1>(w, rws, uo, rec + VOFF + (2 * prs + f) * VFR);
            }
          }
          STAMP(8);
          WAITV(0);
          flag_store(rws, lane < 32 ? lane * 1024 : OOB, WS_F2 + ((hg >> 1) * 32 + rt) * 8, tag, kb);
          STAMP(14);
        }
      }
    }
    const rsrc_t rk = mkrsrc(P.Ak, S * D * 2), rv = mkrsrc(P.Av, S * D * 2), rsh = mkrsrc(P.sh, D * 8), rh = mkrsrc(P.hk, D * S * 2);
    const rsrc_t rq = mkrsrc(P.Aq, S * D * 2), rwo = mkrsrc(P.wo, D * D * 2);
    unsigned l16 = lane * 16, lf = j * 1024 + lane * 16;
    asm volatile("s_waitcnt lgkmcnt(0)" : "+s"(dw[0]), "+s"(dw[1]), "+s"(dw[2]) :: "memory");
    float dv[3];
#pragma unroll
    for (int c = 0; c < 3; c++) dv[c] = __uint_as_float((chn(c) & 1) ? dw[c] & 0xffff0000u : dw[c] << 16);
    unsigned vp[5], vwo[5];
#pragma unroll
    for (int i = 0; i < 3; i++) {
      const unsigned it = 4 * (j + 4 * i) + g, rg = (it * 6554u) >> 16, cc = it - rg * 10;
      const unsigned iq = 4 * (j + 4 * i) + tp, rq_ = (iq * 6554u) >> 16, cq = iq - rq_ * 10;
      vq[i] = iq < 40 ? (64 * rt + 16 * rq_ + (lane >> 2)) * 1280 + 16 * cq : OOB;
      zl[i] = L_ZB + (8 * cc + tq) * 128 + rg * 32 + tp * 8;
    }
#pragma unroll
    for (int i = 0; i < 5; i++) { unsigned u = i * 64 + lane, pr = (u * 6554u) >> 16, ch = u - pr * 10; vwo[i] = (32 * j + pr) * 1280 + 160 * hg + 16 * ch; vp[i] = vwo[i] + 64 * rt * 1280; }
    const int oc = 4 * (j & 1) + g;
    unsigned ka = L_KT - 160 + (8 * oc + 3 - tq) * 160 + (j >> 1) * 32 + tp * 8;
    unsigned wl = L_SH + (16 * (j >> 1) + c16) * 8;
    unsigned so = WS_KVP + (80 * hg + 16 * (j >> 1) + c16) * 4096 + (31 - rt) * 128 + (7 - oc) * 16;
    unsigned la = ((c16 + 1) & 3) * HCS + 16 * g + 8 * ((c16 + 1) >> 2);
    unsigned lb = L_XR + 4064 - 32 * c16 + 16 * g;
    unsigned ld = L_XR + 4088 - 32 * c16 - 8 * g;
    unsigned zo = WS_ZT + 32 * c16 + 8 * g;
    unsigned zi = WS_ZT + (80 * hg + 8 * j + (lane >> 3)) * 4096 + rt * 128 + (lane & 7) * 16;
    unsigned wt0 = (160 * (b >> 6) + 40 * j + (lane >> 1)) * 1280 + 160 * hg + (lane & 1) * 156;
    unsigned fpush = lane < 32 ? lane * 256 : OOB, fpoll = col * 8;
    unsigned f1push = lane < 32 ? WS_F1 + hg * 8192 + lane * 256 + rt * 8 : lane < 40 ? WS_F4 + rt * 1024 + (lane - 32) * 128 + hg * 8 : OOB;
    unsigned f1poll = lane < 32 ? WS_F1 + (hg * 32 + rt) * 256 + lane * 8 : WS_F4 + (rt * 8 + hg) * 128 + (lane & 7) * 8;
    asm volatile("" : "+v"(f1push), "+v"(f1poll), "+v"(wm), "+v"(wr));
    unsigned hsrc = (lane < 4 ? L_H0 : lane < 8 ? L_H1 : L_H2) + 32, hdst = hsrc - 32 + j * HCS + tp * 8;
    asm volatile("" : "+v"(fpush), "+v"(fpoll), "+v"(hsrc), "+v"(hdst));
    unsigned wt1 = lane < 16 ? wt0 + 32 * 1280 : OOB;
    asm volatile("" : "+v"(vp[0]), "+v"(vp[1]), "+v"(vp[2]), "+v"(vp[3]), "+v"(vp[4]), "+v"(vq[0]), "+v"(vq[1]), "+v"(vq[2]), "+v"(zl[0]), "+v"(zl[1]), "+v"(zl[2]), "+v"(zi), "+v"(wt0), "+v"(wt1));
    asm volatile("" : "+v"(vwo[0]), "+v"(vwo[1]), "+v"(vwo[2]), "+v"(vwo[3]), "+v"(vwo[4]), "+v"(ka), "+v"(wl), "+v"(so), "+v"(la), "+v"(lb), "+v"(ld), "+v"(zo), "+v"(l16));
    auto kv_rng = [&](int q, unsigned base, int p0, int n7) {
#pragma unroll 1
      for (int i = p0; i < p0 + 7 * n7; i += 7) {
#pragma unroll
        for (int k = 0; k < 7; k++) dma16c(rws, l16, WS_IMG + q * QRT + 1024 * (i + k), base + 1024 * (i + k));
      }
    };
    if (j == 3) {
      *lp<u32x2>(L_MISC + 4 * W_TAG) = u32x2{tag.lo, tag.hi};
      cnt_wait<1>(C_LAND + 1, NPOLL);
      STAMP(7);
      const unsigned fv = flag_poll2(rws, lane * 8, WS_F2 + (b & 31) * 1024, tag);
      STAMP(8);
      kv_rng(0, L_SA, 0, 1);
      prog_set(C_KM, 1);
      kv_rng(0, L_SA, 7, 1);
      asm volatile("ds_max_u32 %0, %1" :: "v"(L_MISC + 4 * W_KM), "v"(fv << 16) : "memory");
      WAITV(3);
      prog_set(C_KVS, 1);
      cnt_wait4(C_FREE, 7);
    } else {
      if (j == 2) cnt_wait4(C_FREE, 7);
      cnt_wait<0>(C_KM, 1);
      { const u32x2 tw = *lp<u32x2>(L_MISC + 4 * W_TAG); tag.lo = rfl(tw[0]); tag.hi = rfl(tw[1]); }
    }
    __builtin_amdgcn_s_setprio(2);
    STAMP(16);
    auto hcopy = [&](int c, int sg, int p) { dma16(rh, l16, chn(c) * 4096 + 2 * sg + 1024 * p, hbase(c) + sg * HCS + 32 + 1024 * p); };
    if (j >= 2) {
      if (j == 2) kv_rng(0, L_SA, 14, 3); else kv_rng(0, L_SA, 35, 1);
      STAMP(17);
      cnt_wait4(C_FREE, 10);
      kv_rng(1, L_SB, 21 * (j - 2), 1);
      WAITV(7);
      cnt_signal(C_KVS + 1); cnt_signal(C_KVS + 1);
      STAMP(18);
      kv_rng(1, L_SB, 21 * (j - 2) + 7, 2);
      cnt_signal(C_LI);
      STAMP(19);
      WAITV(0);
      cnt_signal(C_KVS + 2); cnt_signal(C_KVS + 2);
#pragma unroll 1
      for (int c = 0; c < 3; c++) {
#pragma unroll
        for (int p = 0; p < 4; p++) hcopy(c, 2 * j - 4, p);
      }
#pragma unroll 1
      for (int c = 0; c < 3; c++) {
#pragma unroll
        for (int p = 0; p < 4; p++) hcopy(c, 2 * j - 3, p);
      }
    } else {
      cnt_wait4(C_FREE, 10);
      cnt_wait<1>(C_LI, 2);
      STAMP(17);
#pragma unroll
      for (int i = 0; i < 5; i++) { dma16(rk, vp[i] - 3840u, 0, L_KT + 5120 * j + 1024 * i); dma16(rv, vp[i], 0, L_VT + 5120 * j + 1024 * i); }
      if (j) dma16(rk, vp[0], 29 * 1280, L_KT + 10240); else dma16(rsh, l16, 640 * hg, L_SH);
      STAMP(18);
      WAITV(0);
      cnt_signal(C_AUX); cnt_signal(C_AUX);
    }
    STAMP(20);
    cnt_wait<3>(C_AUX, 4);
    STAMP(1);
    u32x2 pw[3], r0[3], r1[3], r2[3], v0[3], v1[3];
#pragma unroll
    for (int i = 0; i < 3; i++) {
      pw[i] = *lp<u32x2>(wl + i * 256);
      r0[i] = trd(ka + i * 64 + 1280); r1[i] = trd(ka + i * 64 + 640); r2[i] = trd(ka + i * 64);
      v0[i] = trd(ka + i * 64 + (L_VT - L_KT) + 800); v1[i] = trd(ka + i * 64 + (L_VT - L_KT) + 160);
      SB();
    }
#pragma unroll
    for (int i = 0; i < 3; i++) {
      if (j + 4 * i < 10) {
        const float w0 = lo2f(pw[i][0]), w1 = hi2f(pw[i][0]), w2 = lo2f(pw[i][1]), w3 = hi2f(pw[i][1]);
        const f32x2 p[7] = {{lo2f(r0[i][0]), lo2f(r1[i][0])}, {hi2f(r0[i][0]), hi2f(r1[i][0])}, {lo2f(r0[i][1]), lo2f(r1[i][1])}, {hi2f(r0[i][1]), hi2f(r1[i][1])},
                            {lo2f(r1[i][0]), lo2f(r2[i][0])}, {hi2f(r1[i][0]), hi2f(r2[i][0])}, {lo2f(r1[i][1]), lo2f(r2[i][1])}};
        const f32x2 vv[4] = {{lo2f(v0[i][0]), lo2f(v1[i][0])}, {hi2f(v0[i][0]), hi2f(v1[i][0])}, {lo2f(v0[i][1]), lo2f(v1[i][1])}, {hi2f(v0[i][1]), hi2f(v1[i][1])}};
        f32x2 y[4];
#pragma unroll
        for (int e = 0; e < 4; e++)
#pragma unroll
          for (int z = 0; z < 2; z++) {
            float t;
            asm("v_mul_f32 %0, %1, %2" : "=v"(t) : "v"(p[e + 3][z]), "v"(w3));
            asm("v_fmac_f32 %0, %1, %2" : "+v"(t) : "v"(p[e + 2][z]), "v"(w2));
            asm("v_fmac_f32 %0, %1, %2" : "+v"(t) : "v"(p[e + 1][z]), "v"(w1));
            asm("v_fmac_f32 %0, %1, %2" : "+v"(t) : "v"(p[e][z]), "v"(w0));
            asm("v_mul_f32 %0, %0, %1" : "+v"(t) : "v"(vv[e][z]));
            y[e][z] = t;
          }
        st128<SC1>(u32x4{pk2(y[0][0], y[1][0]), pk2(y[2][0], y[3][0]), pk2(y[0][1], y[1][1]), pk2(y[2][1], y[3][1])}, rws, so, i * 131072);
      }
      if (i == 0) STAMP(21);
    }
#pragma unroll
    for (int i = 0; i < 3; i++) st128<SC1>(u32x4{~0u, ~0u, ~0u, ~0u}, rws, lf, j + 4 * i < 10 ? WS_OX + (rt * 8 + hg) * 10240 + 4096 * i : WS_BYTES);
    STAMP(22);
    WAITV(0);
    if (cnt_arrive(C_F1, lane) == 3) flag_store(rws, f1push, 0, tag);
    STAMP(2);
    if (j == 2) { cnt_wait<3>(C_MP, 4); kv_rng(2, L_SA, 0, 6); WAITV(0); prog_set(C_KVS + 3, 4); }
    int fed = j == 1 ? 0 : 2;
    unsigned mpa = L_MISC + 4 * (C_MP + (j == 1 ? 1 : HURRY - 1));
    auto feed = [&](bool rel) {
      if (fed) { WAITV(0); prog_set(C_KVS + 4, 4); fed = 2; mpa = L_MISC + 4 * (C_MP + HURRY - 1); }
      else if (rel) { kv_rng(3, L_SB, 0, 6); fed = 1; }
    };
    auto mp_ok = [&]() { return rfl(*(volatile LAS unsigned*)(size_t)mpa) >= 4; };
    cnt_wait<4>(C_F1, 4);
    STAMP(23);
    if (lane < 12) {
      const u32x2 hv = *lp<u32x2>(hsrc);
      u32x2 w = {0, 0};
      if (j == 1) w[1] = hv[0] << 16;
      if (j == 2) w[1] = hv[0];
      if (j == 3) { w[0] = hv[0] << 16; w[1] = __builtin_amdgcn_alignbit(hv[1], hv[0], 16); }
      if (tp != 3) w = u32x2{0, 0};
      *lp<u32x2>(hdst) = w;
    }
    if (j < 3 && lane < 32) *lp<u32x4>(L_XR + j * XRC + 4096 + l16) = u32x4{0, 0, 0, 0};
    if (j == 0) { flag_poll<4>(rws, f1poll, 0, tag); prog_set(C_P1, 1); }
    else cnt_wait<3>(C_P1, 1);
    STAMP(24);
    {
#pragma unroll
      for (int c = 0; c < 3; c++) dma16c(rws, lf, WS_KVP + chn(c) * 4096, L_XR + c * XRC + 1024 * j);
      WAITV(0);
      cnt_signal(C_AUX);
    STAMP(25);
    STAMP(26);
      cnt_wait<2>(C_AUX, 8);
    }
    STAMP(4);
    unsigned items = j == 0 ? (7 | 3 << 5) : j == 1 ? (15 | 11 << 5) : j == 3 ? (prod ? (5 | 13 << 5) : (5 | 13 << 5 | 17 << 10))
                            : (prod ? (23 | 1 << 5 | 9 << 10) : (21 | 19 << 5 | 1 << 10 | 9 << 15));
    {
      u32x4 A[8], B[8];
      u32x2 k1, k0;
      unsigned pa, pb;
      auto locate = [&](unsigned it) {
        const int c = (it >> 3) & 3, t = it & 7;
        const unsigned xo = c * XRC - 512 * t;
        pa = la + hbase(c); pb = lb + xo;
        k1 = *lp<u32x2>(ld + xo); k0 = *lp<u32x2>(ld + xo + 512);
      };
      locate(items);
#pragma unroll
      for (int e = 0; e < 8; e++) { B[e] = *lp<u32x4>(pb + 64 * e); SB(); A[e] = lda(pa + 64 * e); SB(); }
      bool hurry = false, first = true;
#pragma unroll 1
      for (;;) {
        const int c = (items >> 3) & 3, t = items & 7;
        const float d = c == 0 ? dv[0] : c == 1 ? dv[1] : dv[2];
        const int ch = chn(c);
        f32x4 acc1 = {smul(d, hi2f(k1[1])), smul(d, lo2f(k1[1])), smul(d, hi2f(k1[0])), smul(d, lo2f(k1[0]))};
        f32x4 acc0 = {smul(d, hi2f(k0[1])), smul(d, lo2f(k0[1])), smul(d, hi2f(k0[0])), smul(d, lo2f(k0[0]))};
        f32x4 bcc1 = {0, 0, 0, 0}, bcc0 = {0, 0, 0, 0};
#pragma unroll 1
        for (int m = 0; m < t; m++) {
          pa += 512; pb += 512;
          asm volatile("s_nop 1" : "+v"(pa), "+v"(pb), "+v"(acc1), "+v"(acc0), "+v"(bcc1), "+v"(bcc0));
          if (!hurry) __builtin_amdgcn_s_setprio(0);
#pragma unroll
          for (int e = 0; e < 8; e++) {
            SB();
            if (e == 0) __builtin_amdgcn_s_waitcnt(lgenc(12));
            if (e == 2) __builtin_amdgcn_s_waitcnt(lgenc(10));
            if (e == 4) __builtin_amdgcn_s_waitcnt(lgenc(8));
            if (e == 6) __builtin_amdgcn_s_waitcnt(lgenc(6));
            SB(); if (e & 1) MF16A(bcc1, A[e], B[e]); else MF16A(acc1, A[e], B[e]);
            SB(); B[e] = *lp<u32x4>(pb + 64 * e);
          }
          SB();
          const unsigned mv = *(volatile LAS unsigned*)(size_t)mpa;
#pragma unroll
          for (int e = 0; e < 8; e++) {
            SB();
            if (e == 0) __builtin_amdgcn_s_waitcnt(lgenc(7));
            if (e == 2) __builtin_amdgcn_s_waitcnt(lgenc(9));
            if (e == 4) __builtin_amdgcn_s_waitcnt(lgenc(11));
            if (e == 6) __builtin_amdgcn_s_waitcnt(lgenc(12));
            SB(); if (e & 1) MF16A(bcc0, A[e], B[e]); else MF16A(acc0, A[e], B[e]);
            SB(); A[e] = lda(pa + 64 * e);
          }
          SB();
          __builtin_amdgcn_s_setprio(2);
          if (!hurry || fed < 2) {
            const bool ok = rfl(mv) >= 4;
            if (fed < 2) { if (HURRY <= 2) hurry = ok; feed(ok); }
            else hurry = ok;
          }
        }
        items >>= 5;
        locate(items ? items : 1u);
        asm volatile("" : "+v"(pa), "+v"(pb));
        if (!hurry) __builtin_amdgcn_s_setprio(0);
#pragma unroll
        for (int e = 0; e < 8; e++) {
          SB();
          if (e == 0) __builtin_amdgcn_s_waitcnt(lgenc(10));
          if (e == 4) __builtin_amdgcn_s_waitcnt(lgenc(12));
          SB(); if (e & 1) MF16A(bcc1, A[e], B[e]); else MF16A(acc1, A[e], B[e]);
          SB(); B[e] = *lp<u32x4>(pb + 64 * e); SB(); A[e] = lda(pa + 64 * e);
        }
        SB();
        __builtin_amdgcn_s_setprio(2);
        asm volatile("s_nop 7" : "+v"(acc1), "+v"(acc0), "+v"(bcc1), "+v"(bcc0));
#pragma unroll
        for (int r = 0; r < 4; r++) {
          asm("v_add_f32 %0, %0, %1" : "+v"(acc0[r]) : "v"(bcc0[r]));
          asm("v_add_f32 %0, %0, %1" : "+v"(acc1[r]) : "v"(bcc1[r]));
        }
        __builtin_amdgcn_raw_buffer_store_b64(u32x2{pk2(acc0[0], acc0[1]), pk2(acc0[2], acc0[3])}, rws, zo, ch * 4096 + 512 * t - 512, SC1);
        __builtin_amdgcn_raw_buffer_store_b64(u32x2{pk2(acc1[0], acc1[1]), pk2(acc1[2], acc1[3])}, rws, zo, ch * 4096 + 512 * t, SC1);
        if (first) {
#pragma unroll
      for (int i = 0; i < 3; i++) dma4(rq, vq[i], 160 * hg, L_JUNK);
      dma4(rwo, wt0, 0, L_JUNK); dma4(rwo, wt1, 0, L_JUNK);
          first = false;
        }
        if (!items) break;
      }
    }
    STAMP(5);
    WAITV(0);
    if (cnt_arrive(C_F3, lane) == 3) flag_store(rws, fpush, WS_F3 + hg * 8192 + rt * 8, tag);
    STAMP(6);
#pragma nounroll
    for (unsigned spins = 0; fed < 2 && spins < SPIN_LDS; ++spins) { feed(mp_ok()); __builtin_amdgcn_s_sleep(4); }
#pragma unroll
    for (int i = 0; i < 3; i++) { qv[i] = __builtin_amdgcn_raw_buffer_load_b128(rq, vq[i], 160 * hg, 0); vq[i] = 2 * vq[i] - (vq[i] & 255); }
    cnt_wait<6>(C_F3, 4);
    cnt_wait<8>(C_MP + 2, 4);
    STAMP(27);
    auto wo_chunk = [&](int c) {
#pragma unroll
      for (int i = 0; i < 5; i++) dma16(rwo, vwo[i], c * 163840, L_WO + c * 20480 + j * 5120 + 1024 * i);
    };
#pragma unroll 1
    for (int c = 0; c < 5; c++) wo_chunk(c);
    STAMP(29);
    WAITV(0);
    cnt_signal(C_WO);
    if (j == 1) { flag_poll<4>(rws, fpoll, WS_F3 + (hg * 32 + rt) * 256, tag); prog_set(C_P3, 1); }
    else cnt_wait<5>(C_P3, 1);
    STAMP(28);
#pragma unroll
    for (int i = 0; i < 3; i++) if (j + 4 * i < 10) dma16c(rws, zi, i * 131072, L_ZB + 4096 * i + 1024 * j);
    WAITV(0);
    grp_bar(C_AUX, 12, lane);
    STAMP(15);
  } else {
    const int rs = wave & 1, kh = wave >> 1;
    unsigned xa[4];
#pragma unroll
    for (int i = 0; i < 4; i++) xa[i] = L_X + (4 * rs + (col >> 3)) * 1024 + (col & 7) * 128 + 16 * ((2 * i + h) ^ ((col >> 1) & 7));
    const unsigned wa = L_WQ + (8 * h + 2 * tq) * 160 + 80 * kh + wpc;
    const int vrow = (col < 18) ? h * 20 + col : (col == 20 || col == 21) ? h * 20 + col - 2 : (col == 18 || col == 22) ? 40 : 41;
    const unsigned ok = lane * 16, ok2 = K2OFF + (h ? 256 : col * 8), ov = VOFF + vrow * 16;
    unsigned pk = L_SA + ok, pk2_ = L_SA + ok2, pv = L_SA + ov;
    const f32x16 zero = {0};
    f32x16 o[2], s[3];
    Rec f;
    int idx = 0xdddd;
    o[0] = zero; o[1] = zero; s[2] = zero; f.v[3] = u32x4{0, 0, 0, 0};
    unsigned ca = L_MISC + 4 * (C_KVS + 2), ma = L_MISC + 4 * (lane ? 48 + lane : C_MP), one = 1;
    asm volatile("" : "+v"(o[0]), "+v"(o[1]), "+v"(s[2]), "+v"(f.v[3]), "+v"(idx), "+v"(pk), "+v"(pk2_), "+v"(pv), "+v"(ca), "+v"(ma), "+v"(one), "+v"(wm), "+v"(wr));
    f32x16 acc0 = zero, acc1 = zero;
    bf16x8 xf[2][4], a0[2][4], a1[2][4];
    int hc = 2 * cc0;
    if (prod) cnt_wait(C_LA, 2);
    u32x2 lv = {0, 0};
#pragma unroll
    for (int c = -1; c < 10; c++) {
      const int z = c & 1, n = z ^ 1;
      const unsigned w0 = wa + ((c + 1) % 5) * SLOT, xo = hc * 8192;
      if (c < 9) {
        if (__builtin_expect(rfl(lv[n]) < (unsigned)((c + 1) >> 1) + 1, 0)) cnt_wait<1>(C_LAND + n, ((c + 1) >> 1) + 1);
        asm volatile("" ::: "memory");
        if (c == -1) STAMP(1);
        if (c == 8) STAMP(15);
        if (c == 2) STAMP(7);
        if (c == 5) STAMP(8);
      }
#pragma unroll
      for (int k = 0; k < 4; k++) {
        SB();
        if (c >= 0) acc0 = MF32(a0[z][k], xf[z][k], acc0);
        SB();
        if (c < 9) { xf[n][k] = *lp<bf16x8>(xa[k] + xo); a0[n][k] = cat2(trd(w0 + k * 2560), trd(w0 + k * 2560 + 160)); }
        SB();
        if (c >= 0) acc1 = MF32(a1[z][k], xf[z][k], acc1);
        SB();
        if (c < 8 && k == 2) lv = *(volatile LAS u32x2*)(size_t)(L_MISC + 4 * C_LAND);
        if (c == 9 && k == 0) lv[0] = *(volatile LAS unsigned*)(size_t)(L_MISC + 4 * C_KVS);
        if (c < 9) a1[n][k] = cat2(trd(w0 + k * 2560 + 40), trd(w0 + k * 2560 + 200));
      }
      if (c < 4 || c == 5 || c == 8) prog_set(C_FREE + wave, c + 2);
      hc = hc == 9 ? 0 : hc + 1;
      SB();
    }
    STAMP(2);
    if (rfl(lv[0]) < 1) cnt_wait<0>(C_KVS, 1);
    asm volatile("" ::: "memory");
    STAMP(4);
    const unsigned kmw = *lp<unsigned>(L_MISC + 4 * W_KM);
    f.k2[0] = *lp<u32x2>(pk2_); f.k1[0] = *lp<u32x4>(pk);
    f.v[0] = *lp<u32x4>(pv); f.v[1] = *lp<u32x4>(pv + VFR); f.k1[1] = *lp<u32x4>(pk + 1024); f.k2[1] = *lp<u32x2>(pk2_ + K2S); SB(); f.v[2] = *lp<u32x4>(pv + 2 * VFR);
    SB();
    bf16x8 q1[2]; s16x4 q2[2];
#pragma unroll
    for (int t = 0; t < 2; t++) {
      const f32x16& a = t ? acc1 : acc0;
      u32x4 w = {pk2(a[0], a[1]), pk2(a[2], a[3]), pk2(a[4], a[5]), pk2(a[6], a[7])};
      u32x2 w2 = {pk2(a[8], a[9]), pk2(a[10], a[11])};
      float n2 = xhalf_sum(dot2(w[0], w[0], dot2(w[1], w[1], 0.f)) + dot2(w[2], w[2], dot2(w[3], w[3], 0.f)));
      n2 = dot2(w2[0], w2[0], dot2(w2[1], w2[1], n2));
      if (h) w2 = u32x2{pk2(__builtin_amdgcn_sqrtf(n2) * __uint_as_float(kmw) * -1.01f, 0.f), 0u};
      q1[t] = asb(w); q2[t] = as4(w2);
    }
    s[0] = MF32(asb(f.k1[0]), q1[0], MF8(as4(f.k2[0]), q2[0], zero));
    STAMP(5);
    {
      int dl = (int)L_SB - (int)L_SA;
      unsigned cv_ = 0, cs_ = 0;
      auto N = []() {};
      int it = 0;
#define REC_(r, h1, h3) lstep(f, s, o, q1, q2, idx, pv + (r) * REC, pk + ((r) + 1) * REC, pk2_ + ((r) + 1) * REC, pv + ((r) + 1) * REC, h1, h3)
#pragma unroll 1
      do {
        STAMPI(16 + it);
        REC_(0, N, [&]() { if (__builtin_expect(it == 0, 0)) cnt_wait<1>(C_KVS + 1, 4); });
        REC_(1, N, N); REC_(2, N, N); REC_(3, N, N); REC_(4, N, N);
        REC_(5, [&]() { cv_ = *(volatile LAS unsigned*)(size_t)ca; }, N);
        REC_(6, [&]() { cs_ = rfl(cv_); }, [&]() { if (__builtin_expect(cs_ < 4, 0)) cnt_wait<1>(C_KVS + 2 + it, 4); asm volatile("" ::: "memory"); });
        lstep(f, s, o, q1, q2, idx, pv + 7 * REC, pk + dl, pk2_ + dl, pv + dl, [&]() { asm volatile("ds_add_u32 %0, %1" :: "v"(ma), "v"(one) : "memory"); }, N);
        pk += dl; pk2_ += dl; pv += dl; dl = -dl;
        ca += 4; ma += 4;
      } while (++it < 4);
      o[1] = PV(asb(f.v[3]), sub8<8>(s[2]), o[1]);
    }
    prog_set(C_OPW + wave, 1);
    STAMP(6);
    if (__builtin_expect(!__all(o[0][10] >= 9.0e-13f && o[1][10] >= 9.0e-13f), 0)) {
      float mx[2] = {-3.0e38f, -3.0e38f};
#pragma unroll
      for (int t = 0; t < 2; t++) { if (h) q2[t] = s16x4{0, 0, 0, 0}; o[t] = zero; }
#pragma unroll 1
      for (int pass = 0; pass < 2; pass++) {
#pragma unroll 1
        for (int r = 0; r < 64; r++) {
          const unsigned so = WS_IMG + (r >> 1) * REC;
          const u32x4 k1 = __builtin_amdgcn_raw_buffer_load_b128(rws, ok, so + (r & 1) * 1024, SC1);
          const u32x2 k2 = __builtin_amdgcn_raw_buffer_load_b64(rws, ok2, so + (r & 1) * K2S, SC1);
          const u32x4 va = __builtin_amdgcn_raw_buffer_load_b128(rws, ov, so + (r & 1) * 2 * VFR, SC1);
          const u32x4 vb = __builtin_amdgcn_raw_buffer_load_b128(rws, ov, so + (r & 1) * 2 * VFR + VFR, SC1);
#pragma unroll
          for (int t = 0; t < 2; t++) {
            f32x16 sv = MF32(asb(k1), q1[t], MF8(as4(k2), q2[t], zero));
            if (pass == 0) {
#pragma unroll
              for (int i = 0; i < 16; i++) mx[t] = fmaxf(mx[t], sv[i]);
            } else {
              exps<0, 16>(sv);
              o[t] = PV(asb(va), sub8<0>(sv), o[t]);
              o[t] = PV(asb(vb), sub8<8>(sv), o[t]);
            }
          }
        }
        if (pass == 0) {
#pragma unroll
          for (int t = 0; t < 2; t++) {
            const float m = -fmaxf(mx[t], __shfl_xor(mx[t], 32));
            const float m0 = hi2f(__float_as_uint(m)), m1 = hi2f(__float_as_uint(m - m0)), m2 = m - m0 - m1;
            if (h) q2[t] = as4(u32x2{pk2(m0, m1), pk2(m2, 0.f)});
          }
        }
      }
    }
    {
      const unsigned ox = WS_OX + ((rt * 8 + hg) * 2 + rs) * 5120;
      u32x2 e;
      auto pkv = [](float a, float b) -> unsigned { return umin(pk2(a, b), 0xfffffffeu); };
#pragma unroll
      for (int t = 0; t < 2; t++) {
        const float inv = __builtin_amdgcn_rcpf(o[t][10]);
        st128<SC1>(u32x4{pkv(o[t][0] * inv, o[t][1] * inv), pkv(o[t][2] * inv, o[t][3] * inv), pkv(o[t][4] * inv, o[t][5] * inv), pkv(o[t][6] * inv, o[t][7] * inv)},
                                               rws, ok, ox + (2 * kh + t) * 1024);
        e[t] = pkv(o[t][8] * inv, o[t][9] * inv);
      }
      __builtin_amdgcn_raw_buffer_store_b64(e, rws, ok, ox + 4096 + 8 * kh, SC1);
    }
    STAMP(10);
    cnt_wait<1>(C_WO, 4);
  }

  {
    constexpr int NPRE = 6;
    auto wfrag = [&](int i, int m) {
      return i >= 2 ? cat2(trd(wm + (i - 2) * 6400 + m * 32), trd(wm + (i - 2) * 6400 + m * 32 + 640))
                    : cat2(trd(wr + i * 25600 + m * 32), trd(wr + i * 25600 + m * 32 + 6400));
    };
    bf16x8 wf[10][5];
#pragma unroll
    for (int i = 0; i < NPRE; i++)
#pragma unroll
      for (int m = 0; m < 5; m++) wf[i][m] = wfrag(i, m);
    SB();
    u32x4 zz[3];
    auto afinal = [&]() {
#pragma unroll
    for (int i = 0; i < 3; i++) {
      if (j + 4 * i < 10) {
        const u32x2 z0 = {zz[i][0], zz[i][1]}, z1 = {zz[i][2], zz[i][3]};
        u32x4 r;
        r[0] = pk2(vmul(lo2f(qv[i][0]), lo2f(z0[0])), vmul(hi2f(qv[i][0]), hi2f(z0[0])));
        r[1] = pk2(vmul(lo2f(qv[i][1]), lo2f(z0[1])), vmul(hi2f(qv[i][1]), hi2f(z0[1])));
        r[2] = pk2(vmul(lo2f(qv[i][2]), lo2f(z1[0])), vmul(hi2f(qv[i][2]), hi2f(z1[0])));
        r[3] = pk2(vmul(lo2f(qv[i][3]), lo2f(z1[1])), vmul(hi2f(qv[i][3]), hi2f(z1[1])));
        st128<0>(r, rout, vq[i], 160 * hg);
      }
    }
    };
    if (okh) {
      const int sl = (tp * 16 + (lane >> 2)) * 4;
#pragma unroll
      for (int i = 0; i < 3; i++) {
        const u32x2 z0 = trd(zl[i]), z1 = trd(zl[i] + 512);
        zz[i] = u32x4{(unsigned)__builtin_amdgcn_ds_bpermute(sl, z0[0]), (unsigned)__builtin_amdgcn_ds_bpermute(sl, z0[1]),
                      (unsigned)__builtin_amdgcn_ds_bpermute(sl, z1[0]), (unsigned)__builtin_amdgcn_ds_bpermute(sl, z1[1])};
      }
      STAMP(9);
      if (AF != 1) cnt_wait<4>(C_OPW + j, 1);
      STAMP(10);
      if (AF != 2) afinal();
      if (AF == 1) cnt_wait<4>(C_OPW + j, 1);
    }
    const unsigned ob = ((g >> 1) * 32 + (r16 & 1) * 16 + c16) * 16, os = WS_OX + ((rt * 8 + 4 * okh) * 2 + (r16 >> 1)) * 5120;
    const unsigned obm = ob + (g & 1) * 1024, obr = ob + (g & 1) * 10240 + 4096;
    auto mx4 = [](u32x4 v) -> unsigned { return umax(umax(v[0], v[1]), umax(v[2], v[3])); };
    u32x4 of[10];
    STAMP(3);
    if (wave == PWAVE) {
      cnt_wait4<1>(C_OPW, 1);
      const unsigned po = (col >> 1) * 5120 + 4096 + (col & 1) * 8 + h * 1012;
#pragma nounroll
      for (unsigned spins = 0; spins < SPIN_GLB; ++spins) {
        asm volatile("" ::: "memory");
        if (__all(__builtin_amdgcn_raw_buffer_load_b32(rws, po, WS_OX + rt * 81920, SC1) != ~0u)) break;
        __builtin_amdgcn_s_sleep(PSL);
      }
      prog_set(C_F4, 1);
    } else cnt_wait<0>(C_F4, 1);
    STAMP(11);
    f32x4 acc[5];
#pragma nounroll
    for (unsigned spins = 0; spins < SPIN_GLB; ++spins) {
      asm volatile("" ::: "memory");
      of[0] = __builtin_amdgcn_raw_buffer_load_b128(rws, obr, os, SC1); SB();
      of[1] = __builtin_amdgcn_raw_buffer_load_b128(rws, obr, os + 20480, SC1); SB();
#pragma unroll
      for (int i = 0; i < 8; i++) { of[2 + i] = __builtin_amdgcn_raw_buffer_load_b128(rws, obm, os + (i >> 1) * 10240 + (i & 1) * 2048, SC1); SB(); }
      bf16x8 wg[4][5];
      unsigned bad = 0;
#pragma unroll
      for (int m = 0; m < 5; m++) acc[m] = f32x4{0, 0, 0, 0};
#pragma unroll
      for (int i = 0; i < 10; i++) {
#pragma unroll
        for (int m = 0; m < 5; m++) {
          SB();
          acc[m] = MF16(i < NPRE ? wf[i][m] : wg[i - NPRE][m], asb(of[i]), acc[m]);
          SB();
          if (i >= 3 && i + 3 < 10) wg[i + 3 - NPRE][m] = wfrag(i + 3, m);
          if (m == 1) bad = umax(bad, mx4(of[i]));
        }
      }
      if (__all(bad != ~0u)) break;
    }
    STAMP(12);
    const unsigned ra = L_RED + okh * 21504 + (16 * r16 + c16) * 336 + g * 16;
#pragma unroll
    for (int m = 0; m < 5; m++) *lp<f32x4>(ra + m * 64) = acc[m];
    WAITL();
    __builtin_amdgcn_s_barrier();
#pragma unroll
    for (int z = 0; z < 2; z++) {
      const unsigned u = z * 64 + lane, row = 8 * wave + ((u * 6554u) >> 16), cc = u - ((u * 6554u) >> 16) * 10;
      if (u < 80) {
        const unsigned a = L_RED + row * 336 + cc * 32;
        const f32x4 y0 = *lp<f32x4>(a) + *lp<f32x4>(a + 21504), y1 = *lp<f32x4>(a + 16) + *lp<f32x4>(a + 21504 + 16);
        st128<0>(u32x4{pk2(y0[0], y0[1]), pk2(y0[2], y0[3]), pk2(y1[0], y1[1]), pk2(y1[2], y1[3])}, rout, (64 * rt + row) * 2560 + 16 * cc, 1280 + 160 * hg);
      }
    }
    if (AF == 2 && okh) afinal();
    if (b == 0 && tid == 448) *(u64*)(P.ws + WS_EPOCH) = ((u64)tag.hi << 16) | (tag.lo >> 16);
    STAMP(13);
    STAMP_OUT();
  }
}

extern "C" void kernel_launch(void* const* d_in, const int* in_sizes, int n_in,
                              void* d_out, int out_size, void* d_ws, size_t ws_size,
                              hipStream_t stream) {
  Params P;
  P.Aq = (const u16*)d_in[0]; P.Ak = (const u16*)d_in[1]; P.Av = (const u16*)d_in[2];
  P.sh = (const u16*)d_in[3]; P.hk = (const u16*)d_in[4]; P.Dv = (const u16*)d_in[5];
  P.x = (const u16*)d_in[6]; P.wq = (const u16*)d_in[7]; P.wk = (const u16*)d_in[8];
  P.wv = (const u16*)d_in[9]; P.wo = (const u16*)d_in[10];
  P.out = (u16*)d_out; P.ws = (char*)d_ws;
  (void)hipFuncSetAttribute((const void*)fused_kernel, hipFuncAttributeMaxDynamicSharedMemorySize, LDS_BYTES);
  fused_kernel<<<256, 512, LDS_BYTES, stream>>>(P);
}
```

```cpp
#include <hip/hip_runtime.h>
#include <hip/hip_bf16.h>

typedef unsigned short u16;
typedef unsigned long long u64;
typedef __attribute__((ext_vector_type(16))) float f32x16;
typedef __attribute__((ext_vector_type(8))) float f32x8;
typedef __attribute__((ext_vector_type(4))) float f32x4;
typedef __attribute__((ext_vector_type(2))) float f32x2;
typedef __attribute__((ext_vector_type(16))) __bf16 bf16x16;
typedef __attribute__((ext_vector_type(8))) __bf16 bf16x8;
typedef __attribute__((ext_vector_type(2))) __bf16 bf16x2;
typedef __attribute__((ext_vector_type(4))) short s16x4;
typedef __attribute__((ext_vector_type(4))) unsigned u32x4;
typedef __attribute__((ext_vector_type(2))) unsigned u32x2;
typedef __amdgpu_buffer_rsrc_t rsrc_t;
#define LAS __attribute__((address_space(3)))
#define GAS __attribute__((address_space(1)))
#define DI __device__ __forceinline__
#define SB() __builtin_amdgcn_sched_barrier(0)

constexpr int S = 2048, D = 640;
constexpr float KSCALE = 0.22360679775f * 1.44269504089f;

constexpr unsigned WS_EPOCH = 0;
constexpr unsigned WS_F1 = 65536;
constexpr unsigned WS_F2 = 131072;
constexpr unsigned WS_F3 = 196608;
constexpr unsigned WS_F4 = 262144;
constexpr unsigned WS_KVP = 1u << 20;
constexpr unsigned WS_IMG = 4u << 20;
constexpr unsigned WS_ZT = 5u << 20;
constexpr unsigned WS_OX = 8u << 20;
constexpr unsigned WS_BYTES = 12u << 20;
constexpr unsigned REC = 5376, K2OFF = 2048, K2S = 320, VOFF = 2688, VFR = 672;

constexpr unsigned L_X = 0, L_WKV = 81920, L_WQ = 107520, SLOT = 10240, L_QX = L_WQ + 2 * SLOT, L_KVR = 158720;
constexpr unsigned QRT = 8 * REC, L_SB = 0, L_SA = 81920;
constexpr unsigned HCS = 4160, L_H0 = 43008, L_H1 = L_H0 + 4 * HCS, L_H2 = 124928, L_SH = 76288, L_JUNK = 77312;
constexpr unsigned L_KT = 141568, L_VT = L_KT + 11264;
constexpr unsigned L_XR = L_KT, XRC = 4608;
constexpr unsigned L_WO = 43008, L_ZB = 145408, L_OP = 0, L_RED = 0;
constexpr unsigned L_MISC = 163328;
constexpr unsigned LDS_BYTES = 163840;
enum { C_MAIN = 0, C_AUX = 1, C_F1 = 2, C_F4 = 3, C_F3 = 4, C_KVD = 5, C_PL = 6, C_KVP = 7, C_LI = 8, C_LA = 9, C_LAND = 10, W_TAG = 12, C_KM = 14, W_KM = 15,
       C_FREE = 16, C_MP = 20, C_OPW = 24, C_P1 = 28, C_P3 = 29, C_AI = 30, C_WO = 31, C_KVS = 32 };
#define PKFMA 0
#define OWIN 8
#define NPRE_ 7
#define AUXA 0
#define AUXB 0
#define AEARLY 0
#define AF 0
#define PWAVE 0
#define PSL 1
#define FLATE 8
#define NPOLL 4
#define HURRY 2

constexpr unsigned CODE_SPAN = 30720;
constexpr int SC1 = 16;
constexpr unsigned OOB = 0x80000000u;
constexpr unsigned SPIN_LDS = 1u << 20, SPIN_GLB = 1u << 17;

struct Params { const u16 *Aq, *Ak, *Av, *sh, *hk, *Dv, *x, *wq, *wk, *wv, *wo; u16* out; char* ws; };

DI unsigned umin(unsigned a, unsigned b) { return a < b ? a : b; }
DI unsigned umax(unsigned a, unsigned b) { return a > b ? a : b; }
DI float lo2f(unsigned v) { return __uint_as_float(v << 16); }
DI float hi2f(unsigned v) { return __uint_as_float(v & 0xffff0000u); }
DI unsigned pk2(float a, float b) { bf16x2 t = {(__bf16)a, (__bf16)b}; return __builtin_bit_cast(unsigned, t); }
DI float dot2(unsigned a, unsigned b, float c) { return __builtin_amdgcn_fdot2_f32_bf16(__builtin_bit_cast(bf16x2, a), __builtin_bit_cast(bf16x2, b), c, false); }
DI float smul(float s, float v) { float r; asm("v_mul_f32 %0, %1, %2" : "=v"(r) : "s"(s), "v"(v)); return r; }
DI float vmul(float a, float b) { float r; asm("v_mul_f32 %0, %1, %2" : "=v"(r) : "v"(a), "v"(b)); return r; }
DI rsrc_t mkrsrc(const void* p, unsigned n) { return __builtin_amdgcn_make_buffer_rsrc((void*)p, 0, n, 0x00020000); }
DI void dma16(rsrc_t rs, unsigned voff, unsigned soff, unsigned lds) {
  asm volatile("s_mov_b32 m0, %0\n\ts_nop 0\n\tbuffer_load_dwordx4 %1, %2, %3 offen lds" :: "s"(lds), "v"(voff), "s"(rs), "s"(soff) : "memory");
}
DI void dma16c(rsrc_t rs, unsigned voff, unsigned soff, unsigned lds) {
  asm volatile("s_mov_b32 m0, %0\n\ts_nop 0\n\tbuffer_load_dwordx4 %1, %2, %3 offen sc1 lds" :: "s"(lds), "v"(voff), "s"(rs), "s"(soff) : "memory");
}
DI void dma4(rsrc_t rs, unsigned voff, unsigned soff, unsigned lds) {
  asm volatile("s_mov_b32 m0, %0\n\ts_nop 0\n\tbuffer_load_dword %1, %2, %3 offen lds" :: "s"(lds), "v"(voff), "s"(rs), "s"(soff) : "memory");
}
template <int AUX> DI void st128(u32x4 v, rsrc_t rs, unsigned voff, unsigned soff) {
  __builtin_amdgcn_raw_buffer_store_b128(v, rs, voff, soff, AUX);
  asm volatile("s_nop 1" :: "v"(v) : "memory");
}
#define WAITV(n) asm volatile("s_waitcnt vmcnt(" #n ")" ::: "memory")
#define WAITL() asm volatile("s_waitcnt lgkmcnt(0)" ::: "memory")
template <typename T> DI LAS T* lp(unsigned a) { return (LAS T*)(size_t)a; }
DI u32x2 trd(unsigned a) { return __builtin_bit_cast(u32x2, __builtin_amdgcn_ds_read_tr16_b64_v4i16(lp<s16x4>(a))); }
DI bf16x8 cat2(u32x2 a, u32x2 b) { return __builtin_bit_cast(bf16x8, __builtin_shufflevector(a, b, 0, 1, 2, 3)); }
DI bf16x8 asb(u32x4 v) { return __builtin_bit_cast(bf16x8, v); }
DI unsigned rfl(unsigned v) { return __builtin_amdgcn_readfirstlane(v); }

struct Cn {
  unsigned mb, one;
  template <int C> DI unsigned arrive() const {
    unsigned r; u64 save;
    asm volatile("s_waitcnt lgkmcnt(0)\n\ts_mov_b64 %1, exec\n\ts_mov_b64 exec, 1\n\tds_add_rtn_u32 %0, %2, %3 offset:%4\n\ts_mov_b64 exec, %1\n\ts_waitcnt lgkmcnt(0)"
                 : "=&v"(r), "=&s"(save) : "v"(mb), "v"(one), "i"(4 * C) : "memory");
    return rfl(r);
  }
  template <int C> DI void signal() const {
    u64 save;
    asm volatile("s_waitcnt lgkmcnt(0)\n\ts_mov_b64 %0, exec\n\ts_mov_b64 exec, 1\n\tds_add_u32 %1, %2 offset:%3\n\ts_mov_b64 exec, %0"
                 : "=&s"(save) : "v"(mb), "v"(one), "i"(4 * C) : "memory");
  }
  template <int SLP> DI void wait(int c, unsigned target) const {
#pragma nounroll
    for (unsigned spins = 0; spins < SPIN_LDS; ++spins) {
      if (rfl(*(volatile LAS unsigned*)(size_t)(mb + 4 * c)) >= target) break;
      __builtin_amdgcn_s_sleep(SLP);
    }
    asm volatile("" ::: "memory");
  }
  template <int SLP> DI void wait4(int c, unsigned target) const {
#pragma nounroll
    for (unsigned spins = 0; spins < SPIN_LDS; ++spins) {
      const u32x4 v = *(volatile LAS u32x4*)(size_t)(mb + 4 * c);
      if (rfl(umin(umin(v[0], v[1]), umin(v[2], v[3]))) >= target) break;
      __builtin_amdgcn_s_sleep(SLP);
    }
    asm volatile("" ::: "memory");
  }
  DI void set(int c, unsigned v) const { asm volatile("" ::: "memory"); *(volatile LAS unsigned*)(size_t)(mb + 4 * c) = v; asm volatile("" ::: "memory"); }
};

struct Tag { unsigned lo, hi; };
DI void flag_store(rsrc_t rs, unsigned voff, unsigned soff, Tag t, unsigned v16 = 0) { __builtin_amdgcn_raw_buffer_store_b64(u32x2{t.lo | v16, t.hi}, rs, voff, soff, SC1); }
DI bool flag_ok(u32x2 v, Tag t) { return (((v[0] ^ t.lo) >> 16) | (v[1] ^ t.hi)) == 0; }
template <int SLP> DI void flag_poll(rsrc_t rs, unsigned voff, unsigned soff, Tag t) {
#pragma nounroll
  for (unsigned spins = 0; spins < SPIN_GLB; ++spins) {
    asm volatile("" ::: "memory");
    const u32x2 v = __builtin_amdgcn_raw_buffer_load_b64(rs, voff, soff, SC1);
    if (__all(flag_ok(v, t))) break;
    __builtin_amdgcn_s_sleep(SLP);
  }
  asm volatile("" ::: "memory");
}
DI unsigned flag_poll2(rsrc_t rs, unsigned voff, unsigned soff, Tag t) {
  u32x2 v = {0, 0};
#pragma nounroll
  for (unsigned spins = 0; spins < SPIN_GLB; ++spins) {
    asm volatile("" ::: "memory");
    v = __builtin_amdgcn_raw_buffer_load_b64(rs, voff, soff, SC1);
    const u32x2 w = __builtin_amdgcn_raw_buffer_load_b64(rs, voff, soff + 512, SC1);
    if (__all(flag_ok(v, t) && flag_ok(w, t))) break;
    __builtin_amdgcn_s_sleep(2);
  }
  asm volatile("" ::: "memory");
  return v[0];
}

#define MF32(a, b, c) __builtin_amdgcn_mfma_f32_32x32x16_bf16(a, b, c, 0, 0, 0)
#define MF8(a, b, c) __builtin_amdgcn_mfma_f32_32x32x8bf16_1k(a, b, c, 0, 0, 0)
#define MF16(a, b, c) __builtin_amdgcn_mfma_f32_16x16x32_bf16(a, b, c, 0, 0, 0)
#define MF16A(c, a, b) asm volatile("v_mfma_f32_16x16x32_bf16 %0, %1, %2, %0" : "+v"(c) : "v"(a), "v"(b))
#define PV(v, p, o) __builtin_amdgcn_smfmac_f32_32x32x32_bf16(v, p, o, idx, 0, 0)

#define STAMP(i)
#define STAMPI(i)
#define STAMP_DECL
#define STAMP_OUT()

template <int O> DI bf16x16 sub8(f32x16 v) {
  f32x8 t = __builtin_shufflevector(v, v, O, O + 1, O + 2, O + 3, O + 4, O + 5, O + 6, O + 7);
  return __builtin_bit_cast(bf16x16, t);
}
template <int A, int B> DI void exps(f32x16& s) {
#pragma unroll
  for (int i = A; i < B; i++) s[i] = __builtin_amdgcn_exp2f(s[i]);
}
DI s16x4 as4(u32x2 v) { return __builtin_bit_cast(s16x4, v); }
struct Rec { u32x4 k1[2]; u32x2 k2[2]; u32x4 v[4]; };
constexpr int vmenc(int n) { return (n & 15) | ((n >> 4) << 14) | (7 << 4) | (15 << 8); }

template <typename F3, typename F4, typename F6>
DI void tile(f32x16& sc, f32x16& sprev, f32x16& oprev, f32x16& ocur, bf16x8 vprev_b, bf16x8 vcur_a,
             bf16x8 kn1, s16x4 kn2, bf16x8 qn1, s16x4 qn2, f32x16& sn, int idx, F3 f3, F4 f4, F6 f6) {
  const f32x16 zero = {0};
  SB(); f32x16 t = MF8(kn2, qn2, zero);
  SB(); exps<0, 4>(sc);
  SB(); t = MF32(kn1, qn1, t);
  SB(); f3();
  SB(); exps<4, 8>(sc);
  SB(); oprev = PV(vprev_b, sub8<8>(sprev), oprev);
  SB(); f4();
  SB(); exps<8, 12>(sc);
  SB(); ocur = PV(vcur_a, sub8<0>(sc), ocur);
  SB(); f6();
  SB(); exps<12, 16>(sc);
  SB(); sn = t;
}
constexpr int lgenc(int n) { return 15 | (3 << 14) | (7 << 4) | (n << 8); }
template <typename H3, typename H6>
DI void lstep(Rec& c, Rec& n, f32x16 (&s)[3], f32x16 (&o)[2], const bf16x8 (&q1)[2], const s16x4 (&q2)[2], int idx,
              unsigned nk, unsigned nk2, unsigned nv, H3 h3, H6 h6) {
#define W_(k) []() { __builtin_amdgcn_s_waitcnt(lgenc(k)); }
#define L_(dst, T, a) [&]() { dst = *lp<T>(a); }
  tile(s[0], s[2], o[1], o[0], asb(n.v[3]), asb(c.v[0]), asb(c.k1[0]), as4(c.k2[0]), q1[1], q2[1], s[1], idx, L_(n.k2[0], u32x2, nk2), L_(n.k1[0], u32x4, nk), W_(3));
  tile(s[1], s[0], o[0], o[1], asb(c.v[1]), asb(c.v[0]), asb(c.k1[1]), as4(c.k2[1]), q1[0], q2[0], s[2], idx, L_(n.v[0], u32x4, nv), L_(n.v[1], u32x4, nv + VFR), L_(n.k2[1], u32x2, nk2 + K2S));
  tile(s[2], s[1], o[1], o[0], asb(c.v[1]), asb(c.v[2]), asb(c.k1[1]), as4(c.k2[1]), q1[1], q2[1], s[0], idx, L_(n.k1[1], u32x4, nk + 1024), L_(n.v[2], u32x4, nv + 2 * VFR), W_(4));
  tile(s[0], s[2], o[0], o[1], asb(c.v[3]), asb(c.v[2]), asb(n.k1[0]), as4(n.k2[0]), q1[0], q2[0], s[1], idx, h3, L_(n.v[3], u32x4, nv + 3 * VFR), h6);
  f32x16 a = s[1], bb = s[0]; s[0] = a; s[2] = bb;
#undef L_
#undef W_
}

DI u32x4 lda(unsigned a) { u32x2 x = *(volatile LAS u32x2*)(size_t)a, y = *(volatile LAS u32x2*)(size_t)(a + 8); return __builtin_shufflevector(x, y, 0, 1, 2, 3); }

template <bool SWAP>
DI void kv_half(f32x16& acc, const unsigned (&xa)[4], unsigned wa, const Cn& cn) {
  bf16x8 xf[2][4], wf[2][4];
#pragma unroll
  for (int i = -1; i < 5; i++) {
    const int l = i + 1;
    if (l < 5) {
      if (l == 0) WAITV(28); else if (l == 1) WAITV(21); else if (l == 2) WAITV(14); else if (l == 3) WAITV(7); else { WAITV(0); cn.signal<C_LA>(); }
#pragma unroll
      for (int k = 0; k < 4; k++) {
        xf[l & 1][k] = *lp<bf16x8>(xa[k] + l * 8192);
        wf[l & 1][k] = cat2(trd(wa + l * 2560 + k * 640), trd(wa + l * 2560 + k * 640 + 160));
      }
    }
    SB();
    if (i >= 0) {
#pragma unroll
      for (int k = 0; k < 4; k++) acc = SWAP ? MF32(xf[i & 1][k], wf[i & 1][k], acc) : MF32(wf[i & 1][k], xf[i & 1][k], acc);
    }
    SB();
  }
}
DI float xhalf_sum(float v) {
  auto r = __builtin_amdgcn_permlane32_swap(__float_as_uint(v), __float_as_uint(v), false, false);
  return __uint_as_float(r[0]) + __uint_as_float(r[1]);
}

__global__ __launch_bounds__(512, 2) void fused_kernel(Params P) {
  extern __shared__ __attribute__((aligned(16))) char smem_raw[];
  const int tid = threadIdx.x, lane = tid & 63;
  const int wave = rfl(tid >> 6);
  const int b = blockIdx.x;
  const int rt = 4 * (b & 7) + (b >> 6), hg = (b >> 3) & 7;
  const bool prod = !((rt + hg) & 1), isV = hg >= 4;
  const int prs = (hg >> 1) & 1;
  const int col = lane & 31, h = lane >> 5, g = lane >> 4, c16 = lane & 15, tq = (lane & 15) >> 2, tp = lane & 3;
  asm volatile("" :: "v"(smem_raw));
  STAMP_DECL;
  STAMP(0);
  unsigned cjunk = 0;
  {
    u64 pc; asm volatile("s_getpc_b64 %0" : "=s"(pc));
    const unsigned o = ((b >> 3) * 8 + tid) * 128;
    if (tid < 8 && o < CODE_SPAN) asm volatile("global_load_dword %0, %1, %2" : "+v"(cjunk) : "v"(o), "s"(pc & ~127ull) : "memory");
  }
  if (tid < 64) *lp<unsigned>(L_MISC + 4 * tid) = tid == C_KVS + 5 ? 2 : 0;
  const int j = wave - 4;
  const int cc0 = (hg + (b >> 6)) % 5;
  unsigned vx[2], vw[5], l16 = lane * 16;
#pragma unroll
  for (int i = 0; i < 2; i++) { int r7 = lane >> 3, pos = lane & 7; vx[i] = (64 * rt + 8 * i + r7) * 1280 + 16 * (pos ^ (4 * i + (r7 >> 1))); }
#pragma unroll
  for (int i = 0; i < 5; i++) { unsigned u = i * 64 + lane, pr = (u * 6554u) >> 16, ch = u - pr * 10, kk = (pr & ~7u) + ((pr & 1) << 2) + ((pr >> 1) & 3); vw[i] = kk * 1280 + 160 * hg + 16 * ch; }
  asm volatile("" : "+v"(vx[0]), "+v"(vx[1]), "+v"(vw[0]), "+v"(vw[1]), "+v"(vw[2]), "+v"(vw[3]), "+v"(vw[4]), "+v"(l16));
  Cn cn = {L_MISC, 1};
  asm volatile("" : "+v"(cn.mb), "+v"(cn.one));
  WAITL();
  const rsrc_t rx = mkrsrc(P.x, S * D * 2);
  u32x2 pw[3];
  {
    const rsrc_t rsh = mkrsrc(P.sh, D * 8);
    const unsigned wo_ = wave >= 4 ? 640 * hg + (16 * ((wave >> 1) & 1) + c16) * 8 : OOB;
    asm volatile("buffer_load_dwordx2 %0, %3, %4, 0 offen\n\tbuffer_load_dwordx2 %1, %3, %4, 0 offen offset:256\n\tbuffer_load_dwordx2 %2, %3, %4, 0 offen offset:512"
                 : "=&v"(pw[0]), "=&v"(pw[1]), "=&v"(pw[2]) : "v"(wo_), "s"(rsh) : "memory");
  }
  auto xhalf = [&](int r, int hc) {
    const unsigned sx = 128 * hc + r * 40960, lx = L_X + hc * 8192 + r * 4096;
    dma16(rx, vx[0], sx, lx); dma16(rx, vx[1], sx, lx + 1024);
    dma16(rx, vx[0], sx + 20480, lx + 2048); dma16(rx, vx[1], sx + 20480, lx + 3072);
  };
  if (__builtin_expect(prod && wave >= 6, 1)) {
    const rsrc_t rwkv = mkrsrc(isV ? P.wv : P.wk, D * 40);
#pragma unroll 1
    for (int i = 5 * (wave - 6); i < 5 * (wave - 5); i++) {
      xhalf(prs, i);
      dma16(rwkv, l16, 2560 * i, L_WKV + 2560 * i);
      dma16(rwkv, l16, 2560 * i + 1024, L_WKV + 2560 * i + 1024);
      if (lane < 32) dma16(rwkv, l16, 2560 * i + 2048, L_WKV + 2560 * i + 2048);
    }
  }
  __builtin_amdgcn_s_barrier();
  const rsrc_t rwq = mkrsrc(P.wq, D * D * 2);
  int lhc = 2 * cc0 + j;
  auto item = [&](int c) {
    const unsigned slot = L_WQ + (c >= 5 ? c - 5 : c) * SLOT;
    if (!prod) xhalf(prs, lhc);
    xhalf(prs ^ 1, lhc);
#pragma unroll
    for (int i = 0; i < 10; i++) dma16(rwq, vw[i % 5], 81920 * lhc + (i / 5) * 40960, slot + 1024 * i);
    lhc = lhc >= 8 ? lhc - 8 : lhc + 2;
  };
  if (__builtin_expect((wave & 6) == 4, 1)) {
    STAMP(7);
#pragma unroll 1
    for (int c = j; c < 10; c += 2) {
      if (c >= 5) cn.wait4<1>(C_FREE, c - 4);
      item(c);
      if (c >= 2) {
        if (prod) WAITV(14); else WAITV(18);
        cn.set(C_LAND + j, c >> 1);
      }
    }
    WAITV(0);
    cn.set(C_LAND + j, 5);
    STAMP(8);
    __builtin_amdgcn_s_setprio(2);
  }
  asm volatile("" ::: "memory");
  const rsrc_t rws = mkrsrc(P.ws, WS_BYTES);
  Tag tag = {0, 0};
  const unsigned wpc = (g & 1) ? 32 : ((tp & 1) * 2 + (tp >> 1)) * 8;
  const rsrc_t rout = mkrsrc(P.out, S * D * 4);

  if (wave >= 4) {
    const int cb = 80 * hg + 5 * (rt >> 1);
    const int cb0 = cb + 3 * (rt & 1);
    auto chn = [=](int c) { return c == 2 ? cb + 2 : cb0 + c; };
    auto hbase = [](int c) { return c == 2 ? L_H2 : L_H0 + c * 4 * HCS; };
    unsigned dw[3];
    asm volatile("s_load_dword %0, %3, %4\n\ts_load_dword %1, %3, %5\n\ts_load_dword %2, %3, %6"
                 : "=&s"(dw[0]), "=&s"(dw[1]), "=&s"(dw[2]) : "s"(P.Dv), "s"((chn(0) >> 1) * 4), "s"((chn(1) >> 1) * 4), "s"((chn(2) >> 1) * 4) : "memory");
    if (j >= 2) {
      __builtin_amdgcn_s_setprio(3);
      const int jj = j - 2;
      u64 epoch;
      asm volatile("s_load_dwordx2 %0, %1, 0x0\n\ts_waitcnt lgkmcnt(0)" : "=s"(epoch) : "s"(P.ws) : "memory");
      epoch += 1;
      tag.lo = (unsigned)epoch << 16; tag.hi = (unsigned)(epoch >> 16);
      if (prod) {
        const int xr = isV ? (col & 19) | ((col & 4) << 1) | ((col & 8) >> 1) : col;
        unsigned xa[4];
#pragma unroll
        for (int i = 0; i < 4; i++) xa[i] = L_X + jj * 40960 + (4 * prs + (xr >> 3)) * 1024 + (xr & 7) * 128 + 16 * ((2 * i + h) ^ ((xr >> 1) & 7));
        const unsigned wa = L_WKV + jj * 12800 + (8 * h + tq) * 40 + wpc;
        f32x16 acc = {0};
        if (isV) kv_half<true>(acc, xa, wa, cn); else kv_half<false>(acc, xa, wa, cn);
        cn.signal<C_KVD>();
        if (!jj) STAMP(7);
        if (jj) {
#pragma unroll
          for (int i = 0; i < 4; i++) *lp<f32x4>(L_KVR + i * 1024 + lane * 16) = f32x4{acc[4 * i], acc[4 * i + 1], acc[4 * i + 2], acc[4 * i + 3]};
          cn.signal<C_KVP>();
        } else {
          cn.wait<0>(C_KVP, 1);
#pragma unroll
          for (int i = 0; i < 4; i++) { f32x4 y = *lp<f32x4>(L_KVR + i * 1024 + lane * 16); acc[4 * i] += y[0]; acc[4 * i + 1] += y[1]; acc[4 * i + 2] += y[2]; acc[4 * i + 3] += y[3]; }
          const unsigned rec = WS_IMG + rt * REC;
          unsigned kb = 0;
          if (!isV) {
            u32x4 w = {pk2(acc[0] * KSCALE, acc[1] * KSCALE), pk2(acc[2] * KSCALE, acc[3] * KSCALE), pk2(acc[4] * KSCALE, acc[5] * KSCALE), pk2(acc[6] * KSCALE, acc[7] * KSCALE)};
            u32x2 w2 = {pk2(acc[8] * KSCALE, acc[9] * KSCALE), pk2(acc[10] * KSCALE, acc[11] * KSCALE)};
            st128<SC1>(w, rws, rec + prs * 1024 + lane * 16, 0);
            __builtin_amdgcn_raw_buffer_store_b64(h ? u32x2{0x3f803f80u, 0x00003f80u} : w2, rws, lane <= 32 ? rec + K2OFF + prs * K2S + lane * 8 : OOB, 0, SC1);
            float n2 = xhalf_sum(dot2(w[0], w[0], dot2(w[1], w[1], dot2(w[2], w[2], dot2(w[3], w[3], 0.f)))));
            n2 = dot2(w2[0], w2[0], dot2(w2[1], w2[1], n2));
#pragma unroll
            for (int o = 16; o; o >>= 1) n2 = __uint_as_float(umax(__float_as_uint(n2), __float_as_uint(__shfl_xor(n2, o))));
            kb = (__float_as_uint(__builtin_amdgcn_sqrtf(n2)) + 0xffffu) >> 16;
          } else {
            const unsigned cv = col == 20 ? 0x3f803f80u : 0u;
            const unsigned uo = col < 20 ? (h * 20 + col) * 16 : (col < 22 && !h) ? (20 + col) * 16 : OOB;
#pragma unroll
            for (int f = 0; f < 2; f++) {
              u32x4 w = {pk2(acc[8 * f], acc[8 * f + 1]), pk2(acc[8 * f + 2], acc[8 * f + 3]), pk2(acc[8 * f + 4], acc[8 * f + 5]), pk2(acc[8 * f + 6], acc[8 * f + 7])};
              if (col >= 20) w = u32x4{cv, cv, cv, cv};
              st128<SC1>(w, rws, uo, rec + VOFF + (2 * prs + f) * VFR);
            }
          }
          STAMP(8);
          WAITV(0);
          flag_store(rws, lane < 32 ? lane * 1024 : OOB, WS_F2 + ((hg >> 1) * 32 + rt) * 8, tag, kb);
          STAMP(14);
        }
      }
    }
    const rsrc_t rk = mkrsrc(P.Ak, S * D * 2), rv = mkrsrc(P.Av, S * D * 2), rh = mkrsrc(P.hk, D * S * 2);
    const rsrc_t rq = mkrsrc(P.Aq, S * D * 2), rwo = mkrsrc(P.wo, D * D * 2);
    unsigned lf = j * 1024 + lane * 16;
    unsigned vp[5], vwo[5], vq[3], zl[3];
#pragma unroll
    for (int i = 0; i < 3; i++) {
      const unsigned it = 4 * (j + 4 * i) + g, rg = (it * 6554u) >> 16, cc = it - rg * 10;
      const unsigned iq = 4 * (j + 4 * i) + tp, rq_ = (iq * 6554u) >> 16, cq = iq - rq_ * 10;
      vq[i] = iq < 40 ? (64 * rt + 16 * rq_ + (lane >> 2)) * 1280 + 16 * cq : OOB;
      zl[i] = L_ZB + (8 * cc + tq) * 128 + rg * 32 + tp * 8;
    }
#pragma unroll
    for (int i = 0; i < 5; i++) { unsigned u = i * 64 + lane, pr = (u * 6554u) >> 16, ch = u - pr * 10; vwo[i] = (32 * j + pr) * 1280 + 160 * hg + 16 * ch; vp[i] = vwo[i] + 64 * rt * 1280; }
    const int oc = 4 * (j & 1) + g;
    unsigned ka = L_KT + (32 * (j & 1) + 8 * tq + 3 - tp + 1 - (g >> 1)) * 160 + (j >> 1) * 32 + (g & 1) * 16;
    unsigned va = L_VT + (8 * oc + 3 - tq) * 160 + (j >> 1) * 32 + tp * 8;
    unsigned so = WS_KVP + (80 * hg + 16 * (j >> 1) + c16) * 4096 + (31 - rt) * 128 + (7 - oc) * 16;
    unsigned la = ((c16 + 1) & 3) * HCS + 16 * g + 8 * ((c16 + 1) >> 2);
    unsigned lb = L_XR + 4064 - 32 * c16 + 16 * g;
    unsigned zo = WS_ZT + 32 * c16 + 8 * g;
    unsigned zi = WS_ZT + (80 * hg + 8 * j + (lane >> 3)) * 4096 + rt * 128 + (lane & 7) * 16;
    unsigned wt0 = (160 * (b >> 6) + 40 * j + (lane >> 1)) * 1280 + 160 * hg + (lane & 1) * 156;
    unsigned fpush = lane < 32 ? lane * 256 : OOB, fpoll = col * 8;
    unsigned f1push = lane < 32 ? WS_F1 + hg * 8192 + lane * 256 + rt * 8 : lane < 40 ? WS_F4 + rt * 1024 + (lane - 32) * 128 + hg * 8 : OOB;
    unsigned f1poll = lane < 32 ? WS_F1 + (hg * 32 + rt) * 256 + lane * 8 : WS_F4 + (rt * 8 + hg) * 128 + (lane & 7) * 8;
    asm volatile("" : "+v"(f1push), "+v"(f1poll));
    unsigned hsrc = (lane < 4 ? L_H0 : lane < 8 ? L_H1 : L_H2) + 32, hdst = hsrc - 32 + j * HCS + tp * 8;
    asm volatile("" : "+v"(fpush), "+v"(fpoll), "+v"(hsrc), "+v"(hdst));
    unsigned wt1 = lane < 16 ? wt0 + 32 * 1280 : OOB;
    asm volatile("" : "+v"(vp[0]), "+v"(vp[1]), "+v"(vp[2]), "+v"(vp[3]), "+v"(vp[4]), "+v"(vq[0]), "+v"(vq[1]), "+v"(vq[2]), "+v"(zl[0]), "+v"(zl[1]), "+v"(zl[2]), "+v"(zi), "+v"(wt0), "+v"(wt1));
    asm volatile("" : "+v"(vwo[0]), "+v"(vwo[1]), "+v"(vwo[2]), "+v"(vwo[3]), "+v"(vwo[4]), "+v"(ka), "+v"(va), "+v"(so), "+v"(la), "+v"(lb), "+v"(zo));
    auto dmask = [](int e) -> u32x4 {
      const unsigned w = (e & 1) ? 0xffff0000u : 0xffffu;
      return u32x4{(e >> 1) == 0 ? w : 0u, (e >> 1) == 1 ? w : 0u, (e >> 1) == 2 ? w : 0u, (e >> 1) == 3 ? w : 0u};
    };
    u32x4 mw_ = dmask(c16 - 8 * (g & 1)), md_ = dmask(15 - c16 - 8 * g);
    unsigned wsel = (g >> 1) ? 0x03020302u : 0x01000100u;
    asm volatile("" : "+v"(mw_), "+v"(md_), "+v"(wsel), "+v"(lf));
    asm volatile("s_waitcnt lgkmcnt(0)" : "+s"(dw[0]), "+s"(dw[1]), "+s"(dw[2]) :: "memory");
    asm volatile("s_waitcnt vmcnt(0)" : "+v"(pw[0]), "+v"(pw[1]), "+v"(pw[2]) :: "memory");
    u32x4 wd[3][2], dd[3];
#pragma unroll
    for (int i = 0; i < 3; i++) {
#pragma unroll
      for (int z = 0; z < 2; z++) { wd[i][z] = mw_ & __builtin_amdgcn_perm(0u, pw[i][z], wsel); asm volatile("" : "+v"(wd[i][z])); }
      dd[i] = md_ & (((chn(i) & 1) ? dw[i] >> 16 : dw[i] & 0xffffu) * 0x10001u); asm volatile("" : "+v"(dd[i]));
    }
    if (j == 2) {
#pragma unroll 1
      for (int i = 0; i < 10; i++) st128<SC1>(u32x4{~0u, ~0u, ~0u, ~0u}, rws, l16, WS_OX + (rt * 8 + hg) * 10240 + 1024 * i);
    }
    auto kv_rng = [&](int q, unsigned base, int p0, int n7) {
#pragma unroll 1
      for (int i = p0; i < p0 + 7 * n7; i += 7) {
#pragma unroll
        for (int k = 0; k < 7; k++) dma16c(rws, l16, WS_IMG + q * QRT + 1024 * (i + k), base + 1024 * (i + k));
      }
    };
    if (j == 3) {
      *lp<u32x2>(L_MISC + 4 * W_TAG) = u32x2{tag.lo, tag.hi};
      cn.wait<1>(C_LAND + 1, NPOLL);
      STAMP(7);
      const unsigned fv = flag_poll2(rws, lane * 8, WS_F2 + (b & 31) * 1024, tag);
      STAMP(8);
      kv_rng(0, L_SA, 0, 1);
      cn.set(C_KM, cn.one);
      kv_rng(0, L_SA, 7, 1);
      asm volatile("ds_max_u32 %0, %1" :: "v"(L_MISC + 4 * W_KM), "v"(fv << 16) : "memory");
      WAITV(3);
      cn.set(C_KVS, cn.one);
      cn.wait4<1>(C_FREE, 7);
    } else {
      if (j == 2) cn.wait4<1>(C_FREE, 7);
      cn.wait<0>(C_KM, 1);
      { const u32x2 tw = *lp<u32x2>(L_MISC + 4 * W_TAG); tag.lo = rfl(tw[0]); tag.hi = rfl(tw[1]); }
    }
    __builtin_amdgcn_s_setprio(2);
    STAMP(16);
    auto hcopy = [&](int c, int sg, int p) { dma16(rh, l16, chn(c) * 4096 + 2 * sg + 1024 * p, hbase(c) + sg * HCS + 32 + 1024 * p); };
    if (j >= 2) {
      if (j == 2) kv_rng(0, L_SA, 14, 3); else kv_rng(0, L_SA, 35, 1);
      STAMP(17);
      cn.wait4<1>(C_FREE, 10);
      kv_rng(1, L_SB, 21 * (j - 2), 1);
      WAITV(7);
      cn.signal<C_KVS + 1>();
      STAMP(18);
      kv_rng(1, L_SB, 21 * (j - 2) + 7, 2);
      cn.signal<C_LI>();
      STAMP(19);
      WAITV(0);
      cn.signal<C_KVS + 2>();
#pragma unroll 1
      for (int c = 0; c < 3; c++) {
#pragma unroll
        for (int p = 0; p < 4; p++) hcopy(c, 2 * j - 4, p);
      }
#pragma unroll 1
      for (int c = 0; c < 3; c++) {
#pragma unroll
        for (int p = 0; p < 4; p++) hcopy(c, 2 * j - 3, p);
      }
    } else {
      cn.wait4<1>(C_FREE, 10);
      cn.wait<1>(C_LI, 2);
      STAMP(17);
#pragma unroll
      for (int i = 0; i < 5; i++) { dma16(rk, vp[i] - 3840u, 0, L_KT + 5120 * j + 1024 * i); dma16(rv, vp[i], 0, L_VT + 5120 * j + 1024 * i); }
      if (j) dma16(rk, vp[0], 29 * 1280, L_KT + 10240);
      STAMP(18);
      WAITV(0);
      cn.signal<C_PL>();
    }
    STAMP(20);
    cn.wait<3>(C_PL, 2);
    STAMP(1);
    {
      u32x4 kf[3][4]; u32x2 v0[3], v1[3];
#pragma unroll
      for (int i = 0; i < 3; i++) {
        kf[i][0] = *lp<u32x4>(ka + i * 64 + 960); kf[i][1] = *lp<u32x4>(ka + i * 64 + 640);
        kf[i][2] = *lp<u32x4>(ka + i * 64 + 320); kf[i][3] = *lp<u32x4>(ka + i * 64);
        v0[i] = trd(va + i * 64 + 640); v1[i] = trd(va + i * 64);
        SB();
      }
      const f32x4 z4 = {0, 0, 0, 0};
      f32x4 ya[3], yb[3];
#pragma unroll
      for (int i = 0; i < 3; i++) {
        if (j + 4 * i < 10) {
          ya[i] = MF16(asb(kf[i][1]), asb(wd[i][1]), MF16(asb(kf[i][0]), asb(wd[i][0]), z4));
          yb[i] = MF16(asb(kf[i][3]), asb(wd[i][1]), MF16(asb(kf[i][2]), asb(wd[i][0]), z4));
          asm volatile("s_nop 7" : "+v"(ya[i]), "+v"(yb[i]));
          st128<SC1>(u32x4{pk2(vmul(ya[i][0], lo2f(v0[i][0])), vmul(ya[i][1], hi2f(v0[i][0]))), pk2(vmul(ya[i][2], lo2f(v0[i][1])), vmul(ya[i][3], hi2f(v0[i][1]))),
                           pk2(vmul(yb[i][0], lo2f(v1[i][0])), vmul(yb[i][1], hi2f(v1[i][0]))), pk2(vmul(yb[i][2], lo2f(v1[i][1])), vmul(yb[i][3], hi2f(v1[i][1])))}, rws, so, i * 131072);
        }
        if (i == 0) STAMP(21);
      }
    }
    STAMP(22);
    WAITV(0);
    if (cn.arrive<C_F1>() == 3) flag_store(rws, f1push, 0, tag);
    STAMP(2);
    if (j == 2) { cn.wait<3>(C_MP, 4); kv_rng(2, L_SA, 0, 6); WAITV(0); cn.set(C_KVS + 3, 2); }
    int fed = j == 1 ? 0 : 2;
    unsigned mpa = L_MISC + 4 * (C_MP + (j == 1 ? 1 : HURRY - 1));
    auto feed = [&](bool rel) {
      if (fed) { WAITV(0); cn.set(C_KVS + 4, 2); fed = 2; mpa = L_MISC + 4 * (C_MP + HURRY - 1); }
      else if (rel) { kv_rng(3, L_SB, 0, 6); fed = 1; }
    };
    auto mp_ok = [&]() { return rfl(*(volatile LAS unsigned*)(size_t)mpa) >= 4; };
    cn.wait<4>(C_F1, 4);
    STAMP(23);
    if (lane < 12) {
      const u32x2 hv = *lp<u32x2>(hsrc);
      u32x2 w = {0, 0};
      if (j == 1) w[1] = hv[0] << 16;
      if (j == 2) w[1] = hv[0];
      if (j == 3) { w[0] = hv[0] << 16; w[1] = __builtin_amdgcn_alignbit(hv[1], hv[0], 16); }
      if (tp != 3) w = u32x2{0, 0};
      *lp<u32x2>(hdst) = w;
    }
    if (j < 3 && lane < 32) *lp<u32x4>(L_XR + j * XRC + 4096 + l16) = u32x4{0, 0, 0, 0};
    if (j == 0) { flag_poll<4>(rws, f1poll, 0, tag); cn.set(C_P1, cn.one); }
    else cn.wait<3>(C_P1, 1);
    STAMP(24);
    {
#pragma unroll
      for (int c = 0; c < 3; c++) dma16c(rws, lf, WS_KVP + chn(c) * 4096, L_XR + c * XRC + 1024 * j);
      WAITV(0);
      cn.signal<C_AUX>();
    STAMP(25);
    STAMP(26);
      cn.wait<2>(C_AUX, 4);
    }
    STAMP(4);
    auto wo_chunk = [&](int c) {
#pragma unroll
      for (int i = 0; i < 5; i++) dma16(rwo, vwo[i], c * 163840, L_WO + c * 20480 + j * 5120 + 1024 * i);
    };
    bool wo23 = false;
    unsigned items = j == 0 ? (7 | 3 << 5) : j == 1 ? (15 | 11 << 5) : j == 3 ? (prod ? (5 | 13 << 5) : (5 | 13 << 5 | 17 << 10))
                            : (prod ? (23 | 1 << 5 | 9 << 10) : (21 | 19 << 5 | 1 << 10 | 9 << 15));
    {
      u32x4 A[8], B[8];
      u32x4 k0;
      unsigned pa, pb;
      auto locate = [&](unsigned it) {
        const int c = (it >> 3) & 3, t = it & 7;
        const unsigned xo = c * XRC - 512 * t;
        pa = la + hbase(c); pb = lb + xo;
        k0 = *lp<u32x4>(pb + 512);
      };
      locate(items);
#pragma unroll
      for (int e = 0; e < 8; e++) { B[e] = *lp<u32x4>(pb + 64 * e); SB(); A[e] = lda(pa + 64 * e); SB(); }
      bool hurry = false, first = true;
#pragma unroll 1
      for (;;) {
        const int c = (items >> 3) & 3, t = items & 7;
        const int ch = chn(c);
        f32x4 acc1, acc0;
#define DINIT(d) asm volatile("v_mfma_f32_16x16x32_bf16 %0, %2, %3, 0\n\tv_mfma_f32_16x16x32_bf16 %1, %2, %4, 0" : "=&v"(acc1), "=&v"(acc0) : "v"(d), "v"(B[0]), "v"(k0))
        if (c == 0) DINIT(dd[0]); else if (c == 1) DINIT(dd[1]); else DINIT(dd[2]);
#pragma unroll 1
        for (int m = 0; m < t; m++) {
          pa += 512; pb += 512;
          asm volatile("" : "+v"(pa), "+v"(pb));
          if (!hurry) __builtin_amdgcn_s_setprio(0);
#pragma unroll
          for (int e = 0; e < 8; e++) {
            SB();
            if (e == 0) __builtin_amdgcn_s_waitcnt(lgenc(12));
            if (e == 2) __builtin_amdgcn_s_waitcnt(lgenc(10));
            if (e == 4) __builtin_amdgcn_s_waitcnt(lgenc(8));
            if (e == 6) __builtin_amdgcn_s_waitcnt(lgenc(6));
            SB(); MF16A(acc1, A[e], B[e]);
            SB(); B[e] = *lp<u32x4>(pb + 64 * e);
          }
          SB();
          const unsigned mv = *(volatile LAS unsigned*)(size_t)mpa;
#pragma unroll
          for (int e = 0; e < 8; e++) {
            SB();
            if (e == 0) __builtin_amdgcn_s_waitcnt(lgenc(7));
            if (e == 2) __builtin_amdgcn_s_waitcnt(lgenc(9));
            if (e == 4) __builtin_amdgcn_s_waitcnt(lgenc(11));
            if (e == 6) __builtin_amdgcn_s_waitcnt(lgenc(12));
            SB(); MF16A(acc0, A[e], B[e]);
            SB(); A[e] = lda(pa + 64 * e);
          }
          SB();
          __builtin_amdgcn_s_setprio(2);
          if (!hurry || fed < 2) {
            const bool ok = rfl(mv) >= 4;
            if (fed < 2) { if (HURRY <= 2) hurry = ok; feed(ok); }
            else { hurry = ok; if (HURRY == 3 && ok) { wo_chunk(2); wo_chunk(3); wo23 = true; } }
          }
        }
        items >>= 5;
        locate(items ? items : 1u);
        asm volatile("" : "+v"(pa), "+v"(pb));
        if (!hurry) __builtin_amdgcn_s_setprio(0);
#pragma unroll
        for (int e = 0; e < 8; e++) {
          SB();
          if (e == 0) __builtin_amdgcn_s_waitcnt(lgenc(9));
          if (e == 4) __builtin_amdgcn_s_waitcnt(lgenc(12));
          SB(); MF16A(acc1, A[e], B[e]);
          SB(); B[e] = *lp<u32x4>(pb + 64 * e); SB(); A[e] = lda(pa + 64 * e);
        }
        SB();
        __builtin_amdgcn_s_setprio(2);
        asm volatile("s_nop 7" : "+v"(acc1), "+v"(acc0));
        __builtin_amdgcn_raw_buffer_store_b64(u32x2{pk2(acc0[0], acc0[1]), pk2(acc0[2], acc0[3])}, rws, zo, ch * 4096 + 512 * t - 512, SC1);
        __builtin_amdgcn_raw_buffer_store_b64(u32x2{pk2(acc1[0], acc1[1]), pk2(acc1[2], acc1[3])}, rws, zo, ch * 4096 + 512 * t, SC1);
        if (first) {
#pragma unroll
      for (int i = 0; i < 3; i++) dma4(rq, vq[i], 160 * hg, L_JUNK);
      dma4(rwo, wt0, 0, L_JUNK); dma4(rwo, wt1, 0, L_JUNK);
          first = false;
        }
        if (!items) break;
      }
    }
    STAMP(5);
    WAITV(0);
    if (cn.arrive<C_F3>() == 3) flag_store(rws, fpush, WS_F3 + hg * 8192 + rt * 8, tag);
    STAMP(6);
#pragma nounroll
    for (unsigned spins = 0; fed < 2 && spins < SPIN_LDS; ++spins) { feed(mp_ok()); __builtin_amdgcn_s_sleep(4); }
    cn.wait<4>(C_F3, 4);
    wo_chunk(0); wo_chunk(4);
    cn.wait<8>(C_MP + 2, 4);
    STAMP(27);
    wo_chunk(1);
    if (!wo23) { wo_chunk(2); wo_chunk(3); }
    u32x4 qv[3];
#pragma unroll
    for (int i = 0; i < 3; i++) qv[i] = __builtin_amdgcn_raw_buffer_load_b128(rq, vq[i], 160 * hg, 0);
    STAMP(29);
    WAITV(3);
    cn.signal<C_WO>();
    STAMP(9);
    if (j == 1) { flag_poll<4>(rws, fpoll, WS_F3 + (hg * 32 + rt) * 256, tag); cn.set(C_P3, cn.one); }
    else cn.wait<5>(C_P3, 1);
    STAMP(28);
#pragma unroll
    for (int i = 0; i < 3; i++) if (j + 4 * i < 10) dma16c(rws, zi, i * 131072, L_ZB + 4096 * i + 1024 * j);
    WAITV(0);
    if (cn.arrive<C_AUX>() < 7) cn.wait<3>(C_AUX, 8);
    STAMP(15);
    if (!AEARLY) cn.wait<4>(C_OPW + j, 1);
    STAMP(10);
    const int sl = (tp * 16 + (lane >> 2)) * 4;
#pragma unroll
    for (int i = 0; i < 3; i++) {
      if (j + 4 * i < 10) {
        const u32x2 y0 = trd(zl[i]), y1 = trd(zl[i] + 512);
        const u32x4 z = {(unsigned)__builtin_amdgcn_ds_bpermute(sl, y0[0]), (unsigned)__builtin_amdgcn_ds_bpermute(sl, y0[1]),
                         (unsigned)__builtin_amdgcn_ds_bpermute(sl, y1[0]), (unsigned)__builtin_amdgcn_ds_bpermute(sl, y1[1])};
        u32x4 r;
#pragma unroll
        for (int e = 0; e < 4; e++) r[e] = pk2(vmul(lo2f(qv[i][e]), lo2f(z[e])), vmul(hi2f(qv[i][e]), hi2f(z[e])));
        st128<AUXA>(r, rout, 2 * vq[i] - (vq[i] & 255), 160 * hg);
      }
    }
    if (b == 0 && j == 3) {
      cn.wait<4>(C_F4, 1);
      if (lane == 0) *(u64*)(P.ws + WS_EPOCH) = ((u64)tag.hi << 16) | (tag.lo >> 16);
    }
  } else {
    const int rs = wave & 1, kh = wave >> 1;
    unsigned xa[4];
#pragma unroll
    for (int i = 0; i < 4; i++) xa[i] = L_X + (4 * rs + (col >> 3)) * 1024 + (col & 7) * 128 + 16 * ((2 * i + h) ^ ((col >> 1) & 7));
    const unsigned wa = L_WQ + (8 * h + 2 * tq) * 160 + 80 * kh + wpc;
    const int vrow = (col < 18) ? h * 20 + col : (col == 20 || col == 21) ? h * 20 + col - 2 : (col == 18 || col == 22) ? 40 : 41;
    const unsigned ok = lane * 16, ok2 = K2OFF + (h ? 256 : col * 8), ov = VOFF + vrow * 16;
    unsigned pk = L_SA + ok, pk2_ = L_SA + ok2, pv = L_SA + ov;
    const f32x16 zero = {0};
    f32x16 o[2], s[3];
    Rec f, f2;
    int idx = 0xdddd;
    o[0] = zero; o[1] = zero; s[2] = zero; f2.v[3] = u32x4{0, 0, 0, 0};
    unsigned wm = L_WO + (20 * (g & 1) + 8 * (g >> 1) + tq) * 160 + tp * 8;
    unsigned wr = L_WO + (80 * (g & 1) + 20 * (tq >> 1) + 16 + 2 * (g >> 1) + (tq & 1)) * 160 + tp * 8;
    unsigned ca = L_MISC + 4 * (C_KVS + 2), ma = L_MISC + 4 * (lane ? 48 + lane : C_MP);
    asm volatile("" : "+v"(o[0]), "+v"(o[1]), "+v"(s[2]), "+v"(f2.v[3]), "+v"(idx), "+v"(pk), "+v"(pk2_), "+v"(pv), "+v"(ca), "+v"(ma), "+v"(wm), "+v"(wr));
    f32x16 acc0 = zero, acc1 = zero;
    bf16x8 xf[2][4], a0[2][4], a1[2][4];
    int hc = 2 * cc0;
    asm volatile("s_waitcnt vmcnt(0)" : "+v"(cjunk), "+v"(pw[0]), "+v"(pw[1]), "+v"(pw[2]) :: "memory");
    if (prod) cn.wait<2>(C_LA, 2);
    u32x2 lv = {0, 0};
#pragma unroll
    for (int c = -1; c < 10; c++) {
      const int z = c & 1, n = z ^ 1;
      const unsigned w0 = wa + ((c + 1) % 5) * SLOT, xo = hc * 8192;
      if (c < 9) {
        if (__builtin_expect(rfl(lv[n]) < (unsigned)((c + 1) >> 1) + 1, 0)) cn.wait<1>(C_LAND + n, ((c + 1) >> 1) + 1);
        asm volatile("" ::: "memory");
        if (c == -1) STAMP(1);
        if (c == 8) STAMP(15);
        if (c == 2) STAMP(7);
        if (c == 5) STAMP(8);
      }
#pragma unroll
      for (int k = 0; k < 4; k++) {
        SB();
        if (c >= 0) acc0 = MF32(a0[z][k], xf[z][k], acc0);
        SB();
        if (c < 9) { xf[n][k] = *lp<bf16x8>(xa[k] + xo); a0[n][k] = cat2(trd(w0 + k * 2560), trd(w0 + k * 2560 + 160)); }
        SB();
        if (c >= 0) acc1 = MF32(a1[z][k], xf[z][k], acc1);
        SB();
        if (c < 8 && k == 2) lv = *(volatile LAS u32x2*)(size_t)(L_MISC + 4 * C_LAND);
        if (c == 9 && k == 0) lv[0] = *(volatile LAS unsigned*)(size_t)(L_MISC + 4 * C_KVS);
        if (c < 9) a1[n][k] = cat2(trd(w0 + k * 2560 + 40), trd(w0 + k * 2560 + 200));
      }
      if (c < 4 || c == 5 || c == 8) cn.set(C_FREE + wave, c + 2);
      hc = hc == 9 ? 0 : hc + 1;
      SB();
    }
    STAMP(2);
    if (rfl(lv[0]) < 1) cn.wait<0>(C_KVS, 1);
    asm volatile("" ::: "memory");
    STAMP(4);
    const unsigned kmw = *lp<unsigned>(L_MISC + 4 * W_KM);
    f.k2[0] = *lp<u32x2>(pk2_); f.k1[0] = *lp<u32x4>(pk);
    f.v[0] = *lp<u32x4>(pv); SB(); f.v[1] = *lp<u32x4>(pv + VFR); f.k1[1] = *lp<u32x4>(pk + 1024); f.k2[1] = *lp<u32x2>(pk2_ + K2S); f.v[2] = *lp<u32x4>(pv + 2 * VFR); SB(); f.v[3] = *lp<u32x4>(pv + 3 * VFR);
    SB();
    bf16x8 q1[2]; s16x4 q2[2];
#pragma unroll
    for (int t = 0; t < 2; t++) {
      const f32x16& a = t ? acc1 : acc0;
      u32x4 w = {pk2(a[0], a[1]), pk2(a[2], a[3]), pk2(a[4], a[5]), pk2(a[6], a[7])};
      u32x2 w2 = {pk2(a[8], a[9]), pk2(a[10], a[11])};
      float n2 = xhalf_sum(dot2(w[0], w[0], dot2(w[1], w[1], 0.f)) + dot2(w[2], w[2], dot2(w[3], w[3], 0.f)));
      n2 = dot2(w2[0], w2[0], dot2(w2[1], w2[1], n2));
      if (h) w2 = u32x2{pk2(__builtin_amdgcn_sqrtf(n2) * __uint_as_float(kmw) * -1.01f, 0.f), 0u};
      q1[t] = asb(w); q2[t] = as4(w2);
    }
    s[0] = MF32(asb(f.k1[0]), q1[0], MF8(as4(f.k2[0]), q2[0], zero));
    STAMP(5);
    {
      int dl = (int)L_SB - (int)L_SA;
      unsigned cv_ = 0, cs_ = 0;
      auto N = []() {};
      int it = 0;
#define REC_(r, c, n, h3, h6) lstep(c, n, s, o, q1, q2, idx, pk + ((r) + 1) * REC, pk2_ + ((r) + 1) * REC, pv + ((r) + 1) * REC, h3, h6)
      __builtin_amdgcn_s_waitcnt(lgenc(5));
#pragma unroll 1
      do {
        STAMPI(16 + it);
        REC_(0, f, f2, N, [&]() { asm volatile("" : "+v"(s[0])); if (__builtin_expect(it == 0, 0)) cn.wait<1>(C_KVS + 1, 2); });
        REC_(1, f2, f, N, N); REC_(2, f, f2, N, N); REC_(3, f2, f, N, N);
        REC_(4, f, f2, [&]() { cv_ = *(volatile LAS unsigned*)(size_t)ca; }, N);
        REC_(5, f2, f, [&]() { cs_ = rfl(cv_); }, N);
        REC_(6, f, f2, [&]() { asm volatile("" : "+v"(s[0])); if (__builtin_expect(cs_ < 2, 0)) cn.wait<1>(C_KVS + 2 + it, 2); asm volatile("" ::: "memory"); },
             [&]() { asm volatile("ds_add_u32 %0, %1" :: "v"(ma), "v"(cn.one) : "memory"); });
        lstep(f2, f, s, o, q1, q2, idx, pk + dl, pk2_ + dl, pv + dl, N, N);
        pk += dl; pk2_ += dl; pv += dl; dl = -dl;
        ca += 4; ma += 4;
      } while (++it < 4);
      o[1] = PV(asb(f2.v[3]), sub8<8>(s[2]), o[1]);
    }
    cn.set(C_OPW + wave, cn.one);
    STAMP(6);
    if (__builtin_expect(!__all(o[0][10] >= 9.0e-13f && o[1][10] >= 9.0e-13f), 0)) {
      float mx[2] = {-3.0e38f, -3.0e38f};
#pragma unroll
      for (int t = 0; t < 2; t++) { if (h) q2[t] = s16x4{0, 0, 0, 0}; o[t] = zero; }
#pragma unroll 1
      for (int pass = 0; pass < 2; pass++) {
#pragma unroll 1
        for (int r = 0; r < 64; r++) {
          const unsigned so = WS_IMG + (r >> 1) * REC;
          const u32x4 k1 = __builtin_amdgcn_raw_buffer_load_b128(rws, ok, so + (r & 1) * 1024, SC1);
          const u32x2 k2 = __builtin_amdgcn_raw_buffer_load_b64(rws, ok2, so + (r & 1) * K2S, SC1);
          const u32x4 va = __builtin_amdgcn_raw_buffer_load_b128(rws, ov, so + (r & 1) * 2 * VFR, SC1);
          const u32x4 vb = __builtin_amdgcn_raw_buffer_load_b128(rws, ov, so + (r & 1) * 2 * VFR + VFR, SC1);
#pragma unroll
          for (int t = 0; t < 2; t++) {
            f32x16 sv = MF32(asb(k1), q1[t], MF8(as4(k2), q2[t], zero));
            if (pass == 0) {
#pragma unroll
              for (int i = 0; i < 16; i++) mx[t] = fmaxf(mx[t], sv[i]);
            } else {
              exps<0, 16>(sv);
              o[t] = PV(asb(va), sub8<0>(sv), o[t]);
              o[t] = PV(asb(vb), sub8<8>(sv), o[t]);
            }
          }
        }
        if (pass == 0) {
#pragma unroll
          for (int t = 0; t < 2; t++) {
            const float m = -fmaxf(mx[t], __shfl_xor(mx[t], 32));
            const float m0 = hi2f(__float_as_uint(m)), m1 = hi2f(__float_as_uint(m - m0)), m2 = m - m0 - m1;
            if (h) q2[t] = as4(u32x2{pk2(m0, m1), pk2(m2, 0.f)});
          }
        }
      }
    }
    {
      const unsigned ox = WS_OX + ((rt * 8 + hg) * 2 + rs) * 5120;
      u32x2 e;
      auto pkv = [](float a, float b) -> unsigned { return umin(pk2(a, b), 0xfffffffeu); };
#pragma unroll
      for (int t = 0; t < 2; t++) {
        const float inv = __builtin_amdgcn_rcpf(o[t][10]);
        st128<SC1>(u32x4{pkv(o[t][0] * inv, o[t][1] * inv), pkv(o[t][2] * inv, o[t][3] * inv), pkv(o[t][4] * inv, o[t][5] * inv), pkv(o[t][6] * inv, o[t][7] * inv)},
                                               rws, ok, ox + (2 * kh + t) * 1024);
        e[t] = pkv(o[t][8] * inv, o[t][9] * inv);
      }
      __builtin_amdgcn_raw_buffer_store_b64(e, rws, ok, ox + 4096 + 8 * kh, SC1);
    }
    STAMP(10);
    constexpr int NPRE = NPRE_;
    unsigned wm2 = wm + 51200, wr2 = wr + 51200;
    asm volatile("" : "+v"(wm2), "+v"(wr2));
    auto wfrag = [&](int i, int m) {
      const unsigned a = i < 8 ? wm + i * 6400 : i < 16 ? wm2 + (i - 8) * 6400 : i < 18 ? wr + (i - 16) * 25600 : wr2 + (i - 18) * 25600;
      const u32x2 x = trd(a + m * 32), y = trd(a + m * 32 + (i < 16 ? 640 : 6400));
      return u32x4{x[0], x[1], y[0], y[1]};
    };
    unsigned fr[3], fo[3];
    const unsigned ra = L_RED + wave * 2816;
#pragma unroll
    for (int z = 0; z < 3; z++) {
      const unsigned u = z * 64 + lane, row = (u * 6554u) >> 16, cc = u - row * 10;
      fr[z] = ra + row * 176 + cc * 16; fo[z] = u < 160 ? (64 * rt + 16 * wave + row) * 2560 + 16 * cc : OOB;
      asm volatile("" : "+v"(fr[z]), "+v"(fo[z]));
    }
    unsigned fw = ra + c16 * 176 + g * 8;
    asm volatile("" : "+v"(fw));
    const unsigned ob = ((g >> 1) * 32 + (wave & 1) * 16 + c16) * 16, os = WS_OX + (rt * 16 + (wave >> 1)) * 5120;
    const unsigned obm = ob + (g & 1) * 1024, obr = ob + (g & 1) * 10240 + 4096;
    const unsigned po = (col >> 1) * 5120 + 4096 + (col & 1) * 8 + h * 1012;
    cn.wait<1>(C_WO, 4);
    f32x4 acc[5];
#pragma nounroll
    for (unsigned tries = 0; tries < SPIN_GLB; ++tries) {
      u32x4 wf[NPRE][5];
#pragma unroll
      for (int i = 0; i < NPRE; i++)
#pragma unroll
        for (int m = 0; m < 5; m++) wf[i][m] = wfrag(i, m);
      SB();
      STAMP(3);
      if (wave == 0) {
        cn.wait4<0>(C_OPW, 1);
#pragma nounroll
        for (unsigned spins = 0; spins < SPIN_GLB; ++spins) {
          asm volatile("" ::: "memory");
          if (__all(__builtin_amdgcn_raw_buffer_load_b32(rws, po, WS_OX + rt * 81920, SC1) != ~0u)) break;
          __builtin_amdgcn_s_sleep(PSL);
        }
        cn.set(C_F4, cn.one);
      } else cn.wait<0>(C_F4, 1);
      STAMP(11);
      u32x4 of[20];
      auto oload = [&](int i) {
        of[i] = i < 16 ? __builtin_amdgcn_raw_buffer_load_b128(rws, obm, os + (i >> 1) * 10240 + (i & 1) * 2048, SC1)
                       : __builtin_amdgcn_raw_buffer_load_b128(rws, obr, os + (i - 16) * 20480, SC1);
      };
#pragma unroll
      for (int i = 0; i < OWIN; i++) { oload(i); SB(); }
      unsigned bad = 0;
#pragma unroll
      for (int i = 0; i < 20; i++) {
#pragma unroll
        for (int m = 0; m < 5; m++) {
          SB();
          if (i == 0) asm volatile("v_mfma_f32_16x16x32_bf16 %0, %1, %2, 0" : "=&v"(acc[m]) : "v"(wf[0][m]), "v"(of[0]));
          else MF16A(acc[m], wf[i % NPRE][m], of[i]);
          SB();
          if (m == 0 && i + OWIN < 20) oload(i + OWIN);
          if (i + NPRE < 20) wf[i % NPRE][m] = wfrag(i + NPRE, m);
          if (m == 3) bad = umax(umax(bad, of[i][0]), of[i][1]);
          if (m == 4) bad = umax(umax(bad, of[i][2]), of[i][3]);
        }
      }
      asm volatile("s_nop 7" : "+v"(acc[0]), "+v"(acc[1]), "+v"(acc[2]), "+v"(acc[3]), "+v"(acc[4]));
      if (__all(bad != ~0u)) break;
    }
    STAMP(12);
#pragma unroll
    for (int m = 0; m < 5; m++) *lp<u32x2>(fw + m * 32) = u32x2{pk2(acc[m][0], acc[m][1]), pk2(acc[m][2], acc[m][3])};
    asm volatile("" ::: "memory");
    u32x4 fy[3];
#pragma unroll
    for (int z = 0; z < 3; z++) fy[z] = *lp<u32x4>(fr[z]);
#pragma unroll
    for (int z = 0; z < 3; z++) st128<AUXB>(fy[z], rout, fo[z], 1280 + 160 * hg);
    STAMP(13);
    STAMP_OUT();
    __builtin_amdgcn_endpgm();
  }
  {
    STAMP(13);
    STAMP_OUT();
  }
}

extern "C" void kernel_launch(void* const* d_in, const int* in_sizes, int n_in,
                              void* d_out, int out_size, void* d_ws, size_t ws_size,
                              hipStream_t stream) {
  Params P;
  P.Aq = (const u16*)d_in[0]; P.Ak = (const u16*)d_in[1]; P.Av = (const u16*)d_in[2];
  P.sh = (const u16*)d_in[3]; P.hk = (const u16*)d_in[4]; P.Dv = (const u16*)d_in[5];
  P.x = (const u16*)d_in[6]; P.wq = (const u16*)d_in[7]; P.wk = (const u16*)d_in[8];
  P.wv = (const u16*)d_in[9]; P.wo = (const u16*)d_in[10];
  P.out = (u16*)d_out; P.ws = (char*)d_ws;
  (void)hipFuncSetAttribute((const void*)fused_kernel, hipFuncAttributeMaxDynamicSharedMemorySize, LDS_BYTES);
  fused_kernel<<<256, 512, LDS_BYTES, stream>>>(P);
}
```

```cpp
#include <hip/hip_runtime.h>
#include <hip/hip_bf16.h>

typedef unsigned short u16;
typedef unsigned long long u64;
typedef __attribute__((ext_vector_type(16))) float f32x16;
typedef __attribute__((ext_vector_type(8))) float f32x8;
typedef __attribute__((ext_vector_type(4))) float f32x4;
typedef __attribute__((ext_vector_type(2))) float f32x2;
typedef __attribute__((ext_vector_type(16))) __bf16 bf16x16;
typedef __attribute__((ext_vector_type(8))) __bf16 bf16x8;
typedef __attribute__((ext_vector_type(2))) __bf16 bf16x2;
typedef __attribute__((ext_vector_type(4))) short s16x4;
typedef __attribute__((ext_vector_type(4))) unsigned u32x4;
typedef __attribute__((ext_vector_type(2))) unsigned u32x2;
typedef __amdgpu_buffer_rsrc_t rsrc_t;
#define LAS __attribute__((address_space(3)))
#define GAS __attribute__((address_space(1)))
#define DI __device__ __forceinline__
#define SB() __builtin_amdgcn_sched_barrier(0)

constexpr int S = 2048, D = 640;
constexpr float KSCALE = 0.22360679775f * 1.44269504089f;

constexpr unsigned WS_EPOCH = 0;
constexpr unsigned WS_F1 = 65536;
constexpr unsigned WS_F2 = 131072;
constexpr unsigned WS_F3 = 196608;
constexpr unsigned WS_F4 = 262144;
constexpr unsigned WS_KVP = 1u << 20;
constexpr unsigned WS_IMG = 4u << 20;
constexpr unsigned WS_ZT = 5u << 20;
constexpr unsigned WS_OX = 8u << 20;
constexpr unsigned WS_BYTES = 12u << 20;
constexpr unsigned REC = 5376, K2OFF = 2048, K2S = 320, VOFF = 2688, VFR = 672;

constexpr unsigned L_X = 0, L_WKV = 81920, L_WQ = 107520, SLOT = 10240, L_QX = L_WQ + 2 * SLOT, L_KVR = 158720;
constexpr unsigned QRT = 8 * REC, L_SB = 0, L_SA = 81920;
constexpr unsigned HCS = 4160, L_H0 = 43008, L_H1 = L_H0 + 4 * HCS, L_H2 = 124928, L_SH = 76288, L_JUNK = 77312;
constexpr unsigned L_KT = 141568, L_VT = L_KT + 11264;
constexpr unsigned L_XR = L_KT, XRC = 4608;
constexpr unsigned L_WO = 43008, L_ZB = 145408, L_OP = 0, L_RED = 0;
constexpr unsigned L_MISC = 163328;
constexpr unsigned LDS_BYTES = 163840;
enum { C_MAIN = 0, C_AUX = 1, C_F1 = 2, C_F4 = 3, C_F3 = 4, C_KVD = 5, C_PL = 6, C_KVP = 7, C_LI = 8, C_LA = 9, C_LAND = 10, W_TAG = 12, C_KM = 14, W_KM = 15,
       C_FREE = 16, C_MP = 20, C_OPW = 24, C_P1 = 28, C_P3 = 29, C_AI = 30, C_WO = 31, C_KVS = 32 };
#define PKFMA 0
#define OWIN 8
#define NPRE_ 7
#define AF 0
#define PWAVE 0
#define PSL 1
#define FLATE 8
#define NPOLL 4
#define HURRY 2

constexpr unsigned CODE_SPAN = 30720;
constexpr int SC1 = 16;
constexpr unsigned OOB = 0x80000000u;
constexpr unsigned SPIN_LDS = 1u << 20, SPIN_GLB = 1u << 17;

struct Params { const u16 *Aq, *Ak, *Av, *sh, *hk, *Dv, *x, *wq, *wk, *wv, *wo; u16* out; char* ws; };

DI unsigned umin(unsigned a, unsigned b) { return a < b ? a : b; }
DI unsigned umax(unsigned a, unsigned b) { return a > b ? a : b; }
DI float lo2f(unsigned v) { return __uint_as_float(v << 16); }
DI float hi2f(unsigned v) { return __uint_as_float(v & 0xffff0000u); }
DI unsigned pk2(float a, float b) { bf16x2 t = {(__bf16)a, (__bf16)b}; return __builtin_bit_cast(unsigned, t); }
DI float dot2(unsigned a, unsigned b, float c) { return __builtin_amdgcn_fdot2_f32_bf16(__builtin_bit_cast(bf16x2, a), __builtin_bit_cast(bf16x2, b), c, false); }
DI float smul(float s, float v) { float r; asm("v_mul_f32 %0, %1, %2" : "=v"(r) : "s"(s), "v"(v)); return r; }
DI float vmul(float a, float b) { float r; asm("v_mul_f32 %0, %1, %2" : "=v"(r) : "v"(a), "v"(b)); return r; }
DI rsrc_t mkrsrc(const void* p, unsigned n) { return __builtin_amdgcn_make_buffer_rsrc((void*)p, 0, n, 0x00020000); }
DI void dma16(rsrc_t rs, unsigned voff, unsigned soff, unsigned lds) {
  asm volatile("s_mov_b32 m0, %0\n\ts_nop 0\n\tbuffer_load_dwordx4 %1, %2, %3 offen lds" :: "s"(lds), "v"(voff), "s"(rs), "s"(soff) : "memory");
}
DI void dma16c(rsrc_t rs, unsigned voff, unsigned soff, unsigned lds) {
  asm volatile("s_mov_b32 m0, %0\n\ts_nop 0\n\tbuffer_load_dwordx4 %1, %2, %3 offen sc1 lds" :: "s"(lds), "v"(voff), "s"(rs), "s"(soff) : "memory");
}
DI void dma4(rsrc_t rs, unsigned voff, unsigned soff, unsigned lds) {
  asm volatile("s_mov_b32 m0, %0\n\ts_nop 0\n\tbuffer_load_dword %1, %2, %3 offen lds" :: "s"(lds), "v"(voff), "s"(rs), "s"(soff) : "memory");
}
template <int AUX> DI void st128(u32x4 v, rsrc_t rs, unsigned voff, unsigned soff) {
  __builtin_amdgcn_raw_buffer_store_b128(v, rs, voff, soff, AUX);
  asm volatile("s_nop 1" :: "v"(v) : "memory");
}
#define WAITV(n) asm volatile("s_waitcnt vmcnt(" #n ")" ::: "memory")
#define WAITL() asm volatile("s_waitcnt lgkmcnt(0)" ::: "memory")
template <typename T> DI LAS T* lp(unsigned a) { return (LAS T*)(size_t)a; }
DI u32x2 trd(unsigned a) { return __builtin_bit_cast(u32x2, __builtin_amdgcn_ds_read_tr16_b64_v4i16(lp<s16x4>(a))); }
DI bf16x8 cat2(u32x2 a, u32x2 b) { return __builtin_bit_cast(bf16x8, __builtin_shufflevector(a, b, 0, 1, 2, 3)); }
DI bf16x8 asb(u32x4 v) { return __builtin_bit_cast(bf16x8, v); }
DI unsigned rfl(unsigned v) { return __builtin_amdgcn_readfirstlane(v); }

struct Cn {
  unsigned mb, one;
  template <int C> DI unsigned arrive() const {
    unsigned r; u64 save;
    asm volatile("s_waitcnt lgkmcnt(0)\n\ts_mov_b64 %1, exec\n\ts_mov_b64 exec, 1\n\tds_add_rtn_u32 %0, %2, %3 offset:%4\n\ts_mov_b64 exec, %1\n\ts_waitcnt lgkmcnt(0)"
                 : "=&v"(r), "=&s"(save) : "v"(mb), "v"(one), "i"(4 * C) : "memory");
    return rfl(r);
  }
  template <int C> DI void signal() const {
    u64 save;
    asm volatile("s_waitcnt lgkmcnt(0)\n\ts_mov_b64 %0, exec\n\ts_mov_b64 exec, 1\n\tds_add_u32 %1, %2 offset:%3\n\ts_mov_b64 exec, %0"
                 : "=&s"(save) : "v"(mb), "v"(one), "i"(4 * C) : "memory");
  }
  template <int SLP> DI void wait(int c, unsigned target) const {
#pragma nounroll
    for (unsigned spins = 0; spins < SPIN_LDS; ++spins) {
      if (rfl(*(volatile LAS unsigned*)(size_t)(mb + 4 * c)) >= target) break;
      __builtin_amdgcn_s_sleep(SLP);
    }
    asm volatile("" ::: "memory");
  }
  template <int SLP> DI void wait4(int c, unsigned target) const {
#pragma nounroll
    for (unsigned spins = 0; spins < SPIN_LDS; ++spins) {
      const u32x4 v = *(volatile LAS u32x4*)(size_t)(mb + 4 * c);
      if (rfl(umin(umin(v[0], v[1]), umin(v[2], v[3]))) >= target) break;
      __builtin_amdgcn_s_sleep(SLP);
    }
    asm volatile("" ::: "memory");
  }
  DI void set(int c, unsigned v) const { asm volatile("" ::: "memory"); *(volatile LAS unsigned*)(size_t)(mb + 4 * c) = v; asm volatile("" ::: "memory"); }
};

struct Tag { unsigned lo, hi; };
DI void flag_store(rsrc_t rs, unsigned voff, unsigned soff, Tag t, unsigned v16 = 0) { __builtin_amdgcn_raw_buffer_store_b64(u32x2{t.lo | v16, t.hi}, rs, voff, soff, SC1); }
DI bool flag_ok(u32x2 v, Tag t) { return (((v[0] ^ t.lo) >> 16) | (v[1] ^ t.hi)) == 0; }
template <int SLP> DI void flag_poll(rsrc_t rs, unsigned voff, unsigned soff, Tag t) {
#pragma nounroll
  for (unsigned spins = 0; spins < SPIN_GLB; ++spins) {
    asm volatile("" ::: "memory");
    const u32x2 v = __builtin_amdgcn_raw_buffer_load_b64(rs, voff, soff, SC1);
    if (__all(flag_ok(v, t))) break;
    __builtin_amdgcn_s_sleep(SLP);
  }
  asm volatile("" ::: "memory");
}
DI unsigned flag_poll2(rsrc_t rs, unsigned voff, unsigned soff, Tag t) {
  u32x2 v = {0, 0};
#pragma nounroll
  for (unsigned spins = 0; spins < SPIN_GLB; ++spins) {
    asm volatile("" ::: "memory");
    v = __builtin_amdgcn_raw_buffer_load_b64(rs, voff, soff, SC1);
    const u32x2 w = __builtin_amdgcn_raw_buffer_load_b64(rs, voff, soff + 512, SC1);
    if (__all(flag_ok(v, t) && flag_ok(w, t))) break;
    __builtin_amdgcn_s_sleep(2);
  }
  asm volatile("" ::: "memory");
  return v[0];
}

#define MF32(a, b, c) __builtin_amdgcn_mfma_f32_32x32x16_bf16(a, b, c, 0, 0, 0)
#define MF8(a, b, c) __builtin_amdgcn_mfma_f32_32x32x8bf16_1k(a, b, c, 0, 0, 0)
#define MF16(a, b, c) __builtin_amdgcn_mfma_f32_16x16x32_bf16(a, b, c, 0, 0, 0)
#define MF16A(c, a, b) asm volatile("v_mfma_f32_16x16x32_bf16 %0, %1, %2, %0" : "+v"(c) : "v"(a), "v"(b))
#define PV(v, p, o) __builtin_amdgcn_smfmac_f32_32x32x32_bf16(v, p, o, idx, 0, 0)

#define STAMP(i)
#define STAMPI(i)
#define STAMP_DECL
#define STAMP_OUT()

template <int O> DI bf16x16 sub8(f32x16 v) {
  f32x8 t = __builtin_shufflevector(v, v, O, O + 1, O + 2, O + 3, O + 4, O + 5, O + 6, O + 7);
  return __builtin_bit_cast(bf16x16, t);
}
template <int A, int B> DI void exps(f32x16& s) {
#pragma unroll
  for (int i = A; i < B; i++) s[i] = __builtin_amdgcn_exp2f(s[i]);
}
DI s16x4 as4(u32x2 v) { return __builtin_bit_cast(s16x4, v); }
struct Rec { u32x4 k1[2]; u32x2 k2[2]; u32x4 v[4]; };
constexpr int vmenc(int n) { return (n & 15) | ((n >> 4) << 14) | (7 << 4) | (15 << 8); }

template <typename F3, typename F4, typename F6>
DI void tile(f32x16& sc, f32x16& sprev, f32x16& oprev, f32x16& ocur, bf16x8 vprev_b, bf16x8 vcur_a,
             bf16x8 kn1, s16x4 kn2, bf16x8 qn1, s16x4 qn2, f32x16& sn, int idx, F3 f3, F4 f4, F6 f6) {
  const f32x16 zero = {0};
  SB(); f32x16 t = MF8(kn2, qn2, zero);
  SB(); exps<0, 4>(sc);
  SB(); t = MF32(kn1, qn1, t);
  SB(); f3();
  SB(); exps<4, 8>(sc);
  SB(); oprev = PV(vprev_b, sub8<8>(sprev), oprev);
  SB(); f4();
  SB(); exps<8, 12>(sc);
  SB(); ocur = PV(vcur_a, sub8<0>(sc), ocur);
  SB(); f6();
  SB(); exps<12, 16>(sc);
  SB(); sn = t;
}
constexpr int lgenc(int n) { return 15 | (3 << 14) | (7 << 4) | (n << 8); }
template <typename H3, typename H6>
DI void lstep(Rec& c, Rec& n, f32x16 (&s)[3], f32x16 (&o)[2], const bf16x8 (&q1)[2], const s16x4 (&q2)[2], int idx,
              unsigned nk, unsigned nk2, unsigned nv, H3 h3, H6 h6) {
#define W_(k) []() { __builtin_amdgcn_s_waitcnt(lgenc(k)); }
#define L_(dst, T, a) [&]() { dst = *lp<T>(a); }
  tile(s[0], s[2], o[1], o[0], asb(n.v[3]), asb(c.v[0]), asb(c.k1[0]), as4(c.k2[0]), q1[1], q2[1], s[1], idx, L_(n.k2[0], u32x2, nk2), L_(n.k1[0], u32x4, nk), W_(3));
  tile(s[1], s[0], o[0], o[1], asb(c.v[1]), asb(c.v[0]), asb(c.k1[1]), as4(c.k2[1]), q1[0], q2[0], s[2], idx, L_(n.v[0], u32x4, nv), L_(n.v[1], u32x4, nv + VFR), L_(n.k2[1], u32x2, nk2 + K2S));
  tile(s[2], s[1], o[1], o[0], asb(c.v[1]), asb(c.v[2]), asb(c.k1[1]), as4(c.k2[1]), q1[1], q2[1], s[0], idx, L_(n.k1[1], u32x4, nk + 1024), L_(n.v[2], u32x4, nv + 2 * VFR), W_(4));
  tile(s[0], s[2], o[0], o[1], asb(c.v[3]), asb(c.v[2]), asb(n.k1[0]), as4(n.k2[0]), q1[0], q2[0], s[1], idx, h3, L_(n.v[3], u32x4, nv + 3 * VFR), h6);
  f32x16 a = s[1], bb = s[0]; s[0] = a; s[2] = bb;
#undef L_
#undef W_
}

DI u32x4 lda(unsigned a) { u32x2 x = *(volatile LAS u32x2*)(size_t)a, y = *(volatile LAS u32x2*)(size_t)(a + 8); return __builtin_shufflevector(x, y, 0, 1, 2, 3); }

template <bool SWAP>
DI void kv_half(f32x16& acc, const unsigned (&xa)[4], unsigned wa, const Cn& cn) {
  bf16x8 xf[2][4], wf[2][4];
#pragma unroll
  for (int i = -1; i < 5; i++) {
    const int l = i + 1;
    if (l < 5) {
      if (l == 0) WAITV(28); else if (l == 1) WAITV(21); else if (l == 2) WAITV(14); else if (l == 3) WAITV(7); else { WAITV(0); cn.signal<C_LA>(); }
#pragma unroll
      for (int k = 0; k < 4; k++) {
        xf[l & 1][k] = *lp<bf16x8>(xa[k] + l * 8192);
        wf[l & 1][k] = cat2(trd(wa + l * 2560 + k * 640), trd(wa + l * 2560 + k * 640 + 160));
      }
    }
    SB();
    if (i >= 0) {
#pragma unroll
      for (int k = 0; k < 4; k++) acc = SWAP ? MF32(xf[i & 1][k], wf[i & 1][k], acc) : MF32(wf[i & 1][k], xf[i & 1][k], acc);
    }
    SB();
  }
}
DI float xhalf_sum(float v) {
  auto r = __builtin_amdgcn_permlane32_swap(__float_as_uint(v), __float_as_uint(v), false, false);
  return __uint_as_float(r[0]) + __uint_as_float(r[1]);
}

__global__ __launch_bounds__(512, 2) void fused_kernel(Params P) {
  extern __shared__ __attribute__((aligned(16))) char smem_raw[];
  const int tid = threadIdx.x, lane = tid & 63;
  const int wave = rfl(tid >> 6);
  const int b = blockIdx.x;
  const int rt = 4 * (b & 7) + (b >> 6), hg = (b >> 3) & 7;
  const bool prod = !((rt + hg) & 1), isV = hg >= 4;
  const int prs = (hg >> 1) & 1;
  const int col = lane & 31, h = lane >> 5, g = lane >> 4, c16 = lane & 15, tq = (lane & 15) >> 2, tp = lane & 3;
  asm volatile("" :: "v"(smem_raw));
  STAMP_DECL;
  STAMP(0);
  unsigned cjunk = 0;
  {
    u64 pc; asm volatile("s_getpc_b64 %0" : "=s"(pc));
    const unsigned o = ((b >> 3) * 8 + tid) * 128;
    if (tid < 8 && o < CODE_SPAN) asm volatile("global_load_dword %0, %1, %2" : "+v"(cjunk) : "v"(o), "s"(pc & ~127ull) : "memory");
  }
  if (tid < 64) *lp<unsigned>(L_MISC + 4 * tid) = tid == C_KVS + 5 ? 2 : 0;
  const int j = wave - 4;
  const int cc0 = (hg + (b >> 6)) % 5;
  unsigned vx[2], vw[5], l16 = lane * 16;
#pragma unroll
  for (int i = 0; i < 2; i++) { int r7 = lane >> 3, pos = lane & 7; vx[i] = (64 * rt + 8 * i + r7) * 1280 + 16 * (pos ^ (4 * i + (r7 >> 1))); }
#pragma unroll
  for (int i = 0; i < 5; i++) { unsigned u = i * 64 + lane, pr = (u * 6554u) >> 16, ch = u - pr * 10, kk = (pr & ~7u) + ((pr & 1) << 2) + ((pr >> 1) & 3); vw[i] = kk * 1280 + 160 * hg + 16 * ch; }
  asm volatile("" : "+v"(vx[0]), "+v"(vx[1]), "+v"(vw[0]), "+v"(vw[1]), "+v"(vw[2]), "+v"(vw[3]), "+v"(vw[4]), "+v"(l16));
  Cn cn = {L_MISC, 1};
  asm volatile("" : "+v"(cn.mb), "+v"(cn.one));
  WAITL();
  const rsrc_t rx = mkrsrc(P.x, S * D * 2);
  u32x2 pw[3];
  {
    const rsrc_t rsh = mkrsrc(P.sh, D * 8);
    const unsigned wo_ = wave >= 4 ? 640 * hg + (16 * ((wave >> 1) & 1) + c16) * 8 : OOB;
    asm volatile("buffer_load_dwordx2 %0, %3, %4, 0 offen\n\tbuffer_load_dwordx2 %1, %3, %4, 0 offen offset:256\n\tbuffer_load_dwordx2 %2, %3, %4, 0 offen offset:512"
                 : "=&v"(pw[0]), "=&v"(pw[1]), "=&v"(pw[2]) : "v"(wo_), "s"(rsh) : "memory");
  }
  auto xhalf = [&](int r, int hc) {
    const unsigned sx = 128 * hc + r * 40960, lx = L_X + hc * 8192 + r * 4096;
    dma16(rx, vx[0], sx, lx); dma16(rx, vx[1], sx, lx + 1024);
    dma16(rx, vx[0], sx + 20480, lx + 2048); dma16(rx, vx[1], sx + 20480, lx + 3072);
  };
  if (__builtin_expect(prod && wave >= 6, 1)) {
    const rsrc_t rwkv = mkrsrc(isV ? P.wv : P.wk, D * 40);
#pragma unroll 1
    for (int i = 5 * (wave - 6); i < 5 * (wave - 5); i++) {
      xhalf(prs, i);
      dma16(rwkv, l16, 2560 * i, L_WKV + 2560 * i);
      dma16(rwkv, l16, 2560 * i + 1024, L_WKV + 2560 * i + 1024);
      if (lane < 32) dma16(rwkv, l16, 2560 * i + 2048, L_WKV + 2560 * i + 2048);
    }
  }
  __builtin_amdgcn_s_barrier();
  const rsrc_t rwq = mkrsrc(P.wq, D * D * 2);
  int lhc = 2 * cc0 + j;
  auto item = [&](int c) {
    const unsigned slot = L_WQ + (c >= 5 ? c - 5 : c) * SLOT;
    if (!prod) xhalf(prs, lhc);
    xhalf(prs ^ 1, lhc);
#pragma unroll
    for (int i = 0; i < 10; i++) dma16(rwq, vw[i % 5], 81920 * lhc + (i / 5) * 40960, slot + 1024 * i);
    lhc = lhc >= 8 ? lhc - 8 : lhc + 2;
  };
  if (__builtin_expect((wave & 6) == 4, 1)) {
    STAMP(7);
#pragma unroll 1
    for (int c = j; c < 10; c += 2) {
      if (c >= 5) cn.wait4<1>(C_FREE, c - 4);
      item(c);
      if (c >= 2) {
        if (prod) WAITV(14); else WAITV(18);
        cn.set(C_LAND + j, c >> 1);
      }
    }
    WAITV(0);
    cn.set(C_LAND + j, 5);
    STAMP(8);
    __builtin_amdgcn_s_setprio(2);
  }
  asm volatile("" ::: "memory");
  const rsrc_t rws = mkrsrc(P.ws, WS_BYTES);
  Tag tag = {0, 0};
  const unsigned wpc = (g & 1) ? 32 : ((tp & 1) * 2 + (tp >> 1)) * 8;
  const rsrc_t rout = mkrsrc(P.out, S * D * 4);

  if (wave >= 4) {
    const int cb = 80 * hg + 5 * (rt >> 1);
    const int cb0 = cb + 3 * (rt & 1);
    auto chn = [=](int c) { return c == 2 ? cb + 2 : cb0 + c; };
    auto hbase = [](int c) { return c == 2 ? L_H2 : L_H0 + c * 4 * HCS; };
    unsigned dw[3];
    asm volatile("s_load_dword %0, %3, %4\n\ts_load_dword %1, %3, %5\n\ts_load_dword %2, %3, %6"
                 : "=&s"(dw[0]), "=&s"(dw[1]), "=&s"(dw[2]) : "s"(P.Dv), "s"((chn(0) >> 1) * 4), "s"((chn(1) >> 1) * 4), "s"((chn(2) >> 1) * 4) : "memory");
    if (j >= 2) {
      __builtin_amdgcn_s_setprio(3);
      const int jj = j - 2;
      u64 epoch;
      asm volatile("s_load_dwordx2 %0, %1, 0x0\n\ts_waitcnt lgkmcnt(0)" : "=s"(epoch) : "s"(P.ws) : "memory");
      epoch += 1;
      tag.lo = (unsigned)epoch << 16; tag.hi = (unsigned)(epoch >> 16);
      if (prod) {
        const int xr = isV ? (col & 19) | ((col & 4) << 1) | ((col & 8) >> 1) : col;
        unsigned xa[4];
#pragma unroll
        for (int i = 0; i < 4; i++) xa[i] = L_X + jj * 40960 + (4 * prs + (xr >> 3)) * 1024 + (xr & 7) * 128 + 16 * ((2 * i + h) ^ ((xr >> 1) & 7));
        const unsigned wa = L_WKV + jj * 12800 + (8 * h + tq) * 40 + wpc;
        f32x16 acc = {0};
        if (isV) kv_half<true>(acc, xa, wa, cn); else kv_half<false>(acc, xa, wa, cn);
        cn.signal<C_KVD>();
        if (!jj) STAMP(7);
        if (jj) {
#pragma unroll
          for (int i = 0; i < 4; i++) *lp<f32x4>(L_KVR + i * 1024 + lane * 16) = f32x4{acc[4 * i], acc[4 * i + 1], acc[4 * i + 2], acc[4 * i + 3]};
          cn.signal<C_KVP>();
        } else {
          cn.wait<0>(C_KVP, 1);
#pragma unroll
          for (int i = 0; i < 4; i++) { f32x4 y = *lp<f32x4>(L_KVR + i * 1024 + lane * 16); acc[4 * i] += y[0]; acc[4 * i + 1] += y[1]; acc[4 * i + 2] += y[2]; acc[4 * i + 3] += y[3]; }
          const unsigned rec = WS_IMG + rt * REC;
          unsigned kb = 0;
          if (!isV) {
            u32x4 w = {pk2(acc[0] * KSCALE, acc[1] * KSCALE), pk2(acc[2] * KSCALE, acc[3] * KSCALE), pk2(acc[4] * KSCALE, acc[5] * KSCALE), pk2(acc[6] * KSCALE, acc[7] * KSCALE)};
            u32x2 w2 = {pk2(acc[8] * KSCALE, acc[9] * KSCALE), pk2(acc[10] * KSCALE, acc[11] * KSCALE)};
            st128<SC1>(w, rws, rec + prs * 1024 + lane * 16, 0);
            __builtin_amdgcn_raw_buffer_store_b64(h ? u32x2{0x3f803f80u, 0x00003f80u} : w2, rws, lane <= 32 ? rec + K2OFF + prs * K2S + lane * 8 : OOB, 0, SC1);
            float n2 = xhalf_sum(dot2(w[0], w[0], dot2(w[1], w[1], dot2(w[2], w[2], dot2(w[3], w[3], 0.f)))));
            n2 = dot2(w2[0], w2[0], dot2(w2[1], w2[1], n2));
#pragma unroll
            for (int o = 16; o; o >>= 1) n2 = __uint_as_float(umax(__float_as_uint(n2), __float_as_uint(__shfl_xor(n2, o))));
            kb = (__float_as_uint(__builtin_amdgcn_sqrtf(n2)) + 0xffffu) >> 16;
          } else {
            const unsigned cv = col == 20 ? 0x3f803f80u : 0u;
            const unsigned uo = col < 20 ? (h * 20 + col) * 16 : (col < 22 && !h) ? (20 + col) * 16 : OOB;
#pragma unroll
            for (int f = 0; f < 2; f++) {
              u32x4 w = {pk2(acc[8 * f], acc[8 * f + 1]), pk2(acc[8 * f + 2], acc[8 * f + 3]), pk2(acc[8 * f + 4], acc[8 * f + 5]), pk2(acc[8 * f + 6], acc[8 * f + 7])};
              if (col >= 20) w = u32x4{cv, cv, cv, cv};
              st128<SC1>(w, rws, uo, rec + VOFF + (2 * prs + f) * VFR);
            }
          }
          STAMP(8);
          WAITV(0);
          flag_store(rws, lane < 32 ? lane * 1024 : OOB, WS_F2 + ((hg >> 1) * 32 + rt) * 8, tag, kb);
          STAMP(14);
        }
      }
    }
    const rsrc_t rk = mkrsrc(P.Ak, S * D * 2), rv = mkrsrc(P.Av, S * D * 2), rh = mkrsrc(P.hk, D * S * 2);
    const rsrc_t rq = mkrsrc(P.Aq, S * D * 2), rwo = mkrsrc(P.wo, D * D * 2);
    unsigned lf = j * 1024 + lane * 16;
    unsigned vp[5], vwo[5], vq[3], zl[3];
#pragma unroll
    for (int i = 0; i < 3; i++) {
      const unsigned it = 4 * (j + 4 * i) + g, rg = (it * 6554u) >> 16, cc = it - rg * 10;
      const unsigned iq = 4 * (j + 4 * i) + tp, rq_ = (iq * 6554u) >> 16, cq = iq - rq_ * 10;
      vq[i] = iq < 40 ? (64 * rt + 16 * rq_ + (lane >> 2)) * 1280 + 16 * cq : OOB;
      zl[i] = L_ZB + (8 * cc + tq) * 128 + rg * 32 + tp * 8;
    }
#pragma unroll
    for (int i = 0; i < 5; i++) { unsigned u = i * 64 + lane, pr = (u * 6554u) >> 16, ch = u - pr * 10; vwo[i] = (32 * j + pr) * 1280 + 160 * hg + 16 * ch; vp[i] = vwo[i] + 64 * rt * 1280; }
    const int oc = 4 * (j & 1) + g;
    unsigned ka = L_KT + (32 * (j & 1) + 8 * tq + 3 - tp + 1 - (g >> 1)) * 160 + (j >> 1) * 32 + (g & 1) * 16;
    unsigned va = L_VT + (8 * oc + 3 - tq) * 160 + (j >> 1) * 32 + tp * 8;
    unsigned so = WS_KVP + (80 * hg + 16 * (j >> 1) + c16) * 4096 + (31 - rt) * 128 + (7 - oc) * 16;
    unsigned la = ((c16 + 1) & 3) * HCS + 16 * g + 8 * ((c16 + 1) >> 2);
    unsigned lb = L_XR + 4064 - 32 * c16 + 16 * g;
    unsigned zo = WS_ZT + 32 * c16 + 8 * g;
    unsigned zi = WS_ZT + (80 * hg + 8 * j + (lane >> 3)) * 4096 + rt * 128 + (lane & 7) * 16;
    unsigned wt0 = (160 * (b >> 6) + 40 * j + (lane >> 1)) * 1280 + 160 * hg + (lane & 1) * 156;
    unsigned fpush = lane < 32 ? lane * 256 : OOB, fpoll = col * 8;
    unsigned f1push = lane < 32 ? WS_F1 + hg * 8192 + lane * 256 + rt * 8 : lane < 40 ? WS_F4 + rt * 1024 + (lane - 32) * 128 + hg * 8 : OOB;
    unsigned f1poll = lane < 32 ? WS_F1 + (hg * 32 + rt) * 256 + lane * 8 : WS_F4 + (rt * 8 + hg) * 128 + (lane & 7) * 8;
    asm volatile("" : "+v"(f1push), "+v"(f1poll));
    unsigned hsrc = (lane < 4 ? L_H0 : lane < 8 ? L_H1 : L_H2) + 32, hdst = hsrc - 32 + j * HCS + tp * 8;
    asm volatile("" : "+v"(fpush), "+v"(fpoll), "+v"(hsrc), "+v"(hdst));
    unsigned wt1 = lane < 16 ? wt0 + 32 * 1280 : OOB;
    asm volatile("" : "+v"(vp[0]), "+v"(vp[1]), "+v"(vp[2]), "+v"(vp[3]), "+v"(vp[4]), "+v"(vq[0]), "+v"(vq[1]), "+v"(vq[2]), "+v"(zl[0]), "+v"(zl[1]), "+v"(zl[2]), "+v"(zi), "+v"(wt0), "+v"(wt1));
    asm volatile("" : "+v"(vwo[0]), "+v"(vwo[1]), "+v"(vwo[2]), "+v"(vwo[3]), "+v"(vwo[4]), "+v"(ka), "+v"(va), "+v"(so), "+v"(la), "+v"(lb), "+v"(zo));
    auto dmask = [](int e) -> u32x4 {
      const unsigned w = (e & 1) ? 0xffff0000u : 0xffffu;
      return u32x4{(e >> 1) == 0 ? w : 0u, (e >> 1) == 1 ? w : 0u, (e >> 1) == 2 ? w : 0u, (e >> 1) == 3 ? w : 0u};
    };
    u32x4 mw_ = dmask(c16 - 8 * (g & 1)), md_ = dmask(15 - c16 - 8 * g);
    unsigned wsel = (g >> 1) ? 0x03020302u : 0x01000100u;
    asm volatile("" : "+v"(mw_), "+v"(md_), "+v"(wsel), "+v"(lf));
    asm volatile("s_waitcnt lgkmcnt(0)" : "+s"(dw[0]), "+s"(dw[1]), "+s"(dw[2]) :: "memory");
    asm volatile("s_waitcnt vmcnt(0)" : "+v"(pw[0]), "+v"(pw[1]), "+v"(pw[2]) :: "memory");
    u32x4 wd[3][2], dd[3];
#pragma unroll
    for (int i = 0; i < 3; i++) {
#pragma unroll
      for (int z = 0; z < 2; z++) { wd[i][z] = mw_ & __builtin_amdgcn_perm(0u, pw[i][z], wsel); asm volatile("" : "+v"(wd[i][z])); }
      dd[i] = md_ & (((chn(i) & 1) ? dw[i] >> 16 : dw[i] & 0xffffu) * 0x10001u); asm volatile("" : "+v"(dd[i]));
    }
    if (j == 2) {
#pragma unroll 1
      for (int i = 0; i < 10; i++) st128<SC1>(u32x4{~0u, ~0u, ~0u, ~0u}, rws, l16, WS_OX + (rt * 8 + hg) * 10240 + 1024 * i);
    }
    auto kv_rng = [&](int q, unsigned base, int p0, int n7) {
#pragma unroll 1
      for (int i = p0; i < p0 + 7 * n7; i += 7) {
#pragma unroll
        for (int k = 0; k < 7; k++) dma16c(rws, l16, WS_IMG + q * QRT + 1024 * (i + k), base + 1024 * (i + k));
      }
    };
    if (j == 3) {
      *lp<u32x2>(L_MISC + 4 * W_TAG) = u32x2{tag.lo, tag.hi};
      cn.wait<1>(C_LAND + 1, NPOLL);
      STAMP(7);
      const unsigned fv = flag_poll2(rws, lane * 8, WS_F2 + (b & 31) * 1024, tag);
      STAMP(8);
      kv_rng(0, L_SA, 0, 1);
      cn.set(C_KM, cn.one);
      kv_rng(0, L_SA, 7, 1);
      asm volatile("ds_max_u32 %0, %1" :: "v"(L_MISC + 4 * W_KM), "v"(fv << 16) : "memory");
      WAITV(3);
      cn.set(C_KVS, cn.one);
      cn.wait4<1>(C_FREE, 7);
    } else {
      if (j == 2) cn.wait4<1>(C_FREE, 7);
      cn.wait<0>(C_KM, 1);
      { const u32x2 tw = *lp<u32x2>(L_MISC + 4 * W_TAG); tag.lo = rfl(tw[0]); tag.hi = rfl(tw[1]); }
    }
    __builtin_amdgcn_s_setprio(2);
    STAMP(16);
    auto hcopy = [&](int c, int sg, int p) { dma16(rh, l16, chn(c) * 4096 + 2 * sg + 1024 * p, hbase(c) + sg * HCS + 32 + 1024 * p); };
    if (j >= 2) {
      if (j == 2) kv_rng(0, L_SA, 14, 3); else kv_rng(0, L_SA, 35, 1);
      STAMP(17);
      cn.wait4<1>(C_FREE, 10);
      kv_rng(1, L_SB, 21 * (j - 2), 1);
      WAITV(7);
      cn.signal<C_KVS + 1>();
      STAMP(18);
      kv_rng(1, L_SB, 21 * (j - 2) + 7, 2);
      cn.signal<C_LI>();
      STAMP(19);
      WAITV(0);
      cn.signal<C_KVS + 2>();
#pragma unroll 1
      for (int c = 0; c < 3; c++) {
#pragma unroll
        for (int p = 0; p < 4; p++) hcopy(c, 2 * j - 4, p);
      }
#pragma unroll 1
      for (int c = 0; c < 3; c++) {
#pragma unroll
        for (int p = 0; p < 4; p++) hcopy(c, 2 * j - 3, p);
      }
    } else {
      cn.wait4<1>(C_FREE, 10);
      cn.wait<1>(C_LI, 2);
      STAMP(17);
#pragma unroll
      for (int i = 0; i < 5; i++) { dma16(rk, vp[i] - 3840u, 0, L_KT + 5120 * j + 1024 * i); dma16(rv, vp[i], 0, L_VT + 5120 * j + 1024 * i); }
      if (j) dma16(rk, vp[0], 29 * 1280, L_KT + 10240);
      STAMP(18);
      WAITV(0);
      cn.signal<C_PL>();
    }
    STAMP(20);
    cn.wait<3>(C_PL, 2);
    STAMP(1);
    {
      u32x4 kf[3][4]; u32x2 v0[3], v1[3];
#pragma unroll
      for (int i = 0; i < 3; i++) {
        kf[i][0] = *lp<u32x4>(ka + i * 64 + 960); kf[i][1] = *lp<u32x4>(ka + i * 64 + 640);
        kf[i][2] = *lp<u32x4>(ka + i * 64 + 320); kf[i][3] = *lp<u32x4>(ka + i * 64);
        v0[i] = trd(va + i * 64 + 640); v1[i] = trd(va + i * 64);
        SB();
      }
      const f32x4 z4 = {0, 0, 0, 0};
      f32x4 ya[3], yb[3];
#pragma unroll
      for (int i = 0; i < 3; i++) {
        if (j + 4 * i < 10) {
          ya[i] = MF16(asb(kf[i][1]), asb(wd[i][1]), MF16(asb(kf[i][0]), asb(wd[i][0]), z4));
          yb[i] = MF16(asb(kf[i][3]), asb(wd[i][1]), MF16(asb(kf[i][2]), asb(wd[i][0]), z4));
          asm volatile("s_nop 7" : "+v"(ya[i]), "+v"(yb[i]));
          st128<SC1>(u32x4{pk2(vmul(ya[i][0], lo2f(v0[i][0])), vmul(ya[i][1], hi2f(v0[i][0]))), pk2(vmul(ya[i][2], lo2f(v0[i][1])), vmul(ya[i][3], hi2f(v0[i][1]))),
                           pk2(vmul(yb[i][0], lo2f(v1[i][0])), vmul(yb[i][1], hi2f(v1[i][0]))), pk2(vmul(yb[i][2], lo2f(v1[i][1])), vmul(yb[i][3], hi2f(v1[i][1])))}, rws, so, i * 131072);
        }
        if (i == 0) STAMP(21);
      }
    }
    STAMP(22);
    WAITV(0);
    if (cn.arrive<C_F1>() == 3) flag_store(rws, f1push, 0, tag);
    STAMP(2);
    if (j == 2) { cn.wait<3>(C_MP, 4); kv_rng(2, L_SA, 0, 6); WAITV(0); cn.set(C_KVS + 3, 2); }
    int fed = j == 1 ? 0 : 2;
    unsigned mpa = L_MISC + 4 * (C_MP + (j == 1 ? 1 : HURRY - 1));
    auto feed = [&](bool rel) {
      if (fed) { WAITV(0); cn.set(C_KVS + 4, 2); fed = 2; mpa = L_MISC + 4 * (C_MP + HURRY - 1); }
      else if (rel) { kv_rng(3, L_SB, 0, 6); fed = 1; }
    };
    auto mp_ok = [&]() { return rfl(*(volatile LAS unsigned*)(size_t)mpa) >= 4; };
    cn.wait<4>(C_F1, 4);
    STAMP(23);
    if (lane < 12) {
      const u32x2 hv = *lp<u32x2>(hsrc);
      u32x2 w = {0, 0};
      if (j == 1) w[1] = hv[0] << 16;
      if (j == 2) w[1] = hv[0];
      if (j == 3) { w[0] = hv[0] << 16; w[1] = __builtin_amdgcn_alignbit(hv[1], hv[0], 16); }
      if (tp != 3) w = u32x2{0, 0};
      *lp<u32x2>(hdst) = w;
    }
    if (j < 3 && lane < 32) *lp<u32x4>(L_XR + j * XRC + 4096 + l16) = u32x4{0, 0, 0, 0};
    if (j == 0) { flag_poll<4>(rws, f1poll, 0, tag); cn.set(C_P1, cn.one); }
    else cn.wait<3>(C_P1, 1);
    STAMP(24);
    {
#pragma unroll
      for (int c = 0; c < 3; c++) dma16c(rws, lf, WS_KVP + chn(c) * 4096, L_XR + c * XRC + 1024 * j);
      WAITV(0);
      cn.signal<C_AUX>();
    STAMP(25);
    STAMP(26);
      cn.wait<2>(C_AUX, 4);
    }
    STAMP(4);
    auto wo_chunk = [&](int c) {
#pragma unroll
      for (int i = 0; i < 5; i++) dma16(rwo, vwo[i], c * 163840, L_WO + c * 20480 + j * 5120 + 1024 * i);
    };
    bool wo23 = false;
    unsigned items = j == 0 ? (7 | 3 << 5) : j == 1 ? (15 | 11 << 5) : j == 3 ? (prod ? (5 | 13 << 5) : (5 | 13 << 5 | 17 << 10))
                            : (prod ? (23 | 1 << 5 | 9 << 10) : (21 | 19 << 5 | 1 << 10 | 9 << 15));
    {
      u32x4 A[8], B[8];
      u32x4 k0;
      unsigned pa, pb;
      auto locate = [&](unsigned it) {
        const int c = (it >> 3) & 3, t = it & 7;
        const unsigned xo = c * XRC - 512 * t;
        pa = la + hbase(c); pb = lb + xo;
        k0 = *lp<u32x4>(pb + 512);
      };
      locate(items);
#pragma unroll
      for (int e = 0; e < 8; e++) { B[e] = *lp<u32x4>(pb + 64 * e); SB(); A[e] = lda(pa + 64 * e); SB(); }
      bool hurry = false, first = true;
#pragma unroll 1
      for (;;) {
        const int c = (items >> 3) & 3, t = items & 7;
        const int ch = chn(c);
        f32x4 acc1, acc0;
#define DINIT(d) asm volatile("v_mfma_f32_16x16x32_bf16 %0, %2, %3, 0\n\tv_mfma_f32_16x16x32_bf16 %1, %2, %4, 0" : "=&v"(acc1), "=&v"(acc0) : "v"(d), "v"(B[0]), "v"(k0))
        if (c == 0) DINIT(dd[0]); else if (c == 1) DINIT(dd[1]); else DINIT(dd[2]);
#pragma unroll 1
        for (int m = 0; m < t; m++) {
          pa += 512; pb += 512;
          asm volatile("" : "+v"(pa), "+v"(pb));
          if (!hurry) __builtin_amdgcn_s_setprio(0);
#pragma unroll
          for (int e = 0; e < 8; e++) {
            SB();
            if (e == 0) __builtin_amdgcn_s_waitcnt(lgenc(12));
            if (e == 2) __builtin_amdgcn_s_waitcnt(lgenc(10));
            if (e == 4) __builtin_amdgcn_s_waitcnt(lgenc(8));
            if (e == 6) __builtin_amdgcn_s_waitcnt(lgenc(6));
            SB(); MF16A(acc1, A[e], B[e]);
            SB(); B[e] = *lp<u32x4>(pb + 64 * e);
          }
          SB();
          const unsigned mv = *(volatile LAS unsigned*)(size_t)mpa;
#pragma unroll
          for (int e = 0; e < 8; e++) {
            SB();
            if (e == 0) __builtin_amdgcn_s_waitcnt(lgenc(7));
            if (e == 2) __builtin_amdgcn_s_waitcnt(lgenc(9));
            if (e == 4) __builtin_amdgcn_s_waitcnt(lgenc(11));
            if (e == 6) __builtin_amdgcn_s_waitcnt(lgenc(12));
            SB(); MF16A(acc0, A[e], B[e]);
            SB(); A[e] = lda(pa + 64 * e);
          }
          SB();
          __builtin_amdgcn_s_setprio(2);
          if (!hurry || fed < 2) {
            const bool ok = rfl(mv) >= 4;
            if (fed < 2) { if (HURRY <= 2) hurry = ok; feed(ok); }
            else { hurry = ok; if (HURRY == 3 && ok) { wo_chunk(2); wo_chunk(3); wo23 = true; } }
          }
        }
        items >>= 5;
        locate(items ? items : 1u);
        asm volatile("" : "+v"(pa), "+v"(pb));
        if (!hurry) __builtin_amdgcn_s_setprio(0);
#pragma unroll
        for (int e = 0; e < 8; e++) {
          SB();
          if (e == 0) __builtin_amdgcn_s_waitcnt(lgenc(9));
          if (e == 4) __builtin_amdgcn_s_waitcnt(lgenc(12));
          SB(); MF16A(acc1, A[e], B[e]);
          SB(); B[e] = *lp<u32x4>(pb + 64 * e); SB(); A[e] = lda(pa + 64 * e);
        }
        SB();
        __builtin_amdgcn_s_setprio(2);
        asm volatile("s_nop 7" : "+v"(acc1), "+v"(acc0));
        __builtin_amdgcn_raw_buffer_store_b64(u32x2{pk2(acc0[0], acc0[1]), pk2(acc0[2], acc0[3])}, rws, zo, ch * 4096 + 512 * t - 512, SC1);
        __builtin_amdgcn_raw_buffer_store_b64(u32x2{pk2(acc1[0], acc1[1]), pk2(acc1[2], acc1[3])}, rws, zo, ch * 4096 + 512 * t, SC1);
        if (first) {
#pragma unroll
      for (int i = 0; i < 3; i++) dma4(rq, vq[i], 160 * hg, L_JUNK);
      dma4(rwo, wt0, 0, L_JUNK); dma4(rwo, wt1, 0, L_JUNK);
          first = false;
        }
        if (!items) break;
      }
    }
    STAMP(5);
    WAITV(0);
    if (cn.arrive<C_F3>() == 3) flag_store(rws, fpush, WS_F3 + hg * 8192 + rt * 8, tag);
    STAMP(6);
#pragma nounroll
    for (unsigned spins = 0; fed < 2 && spins < SPIN_LDS; ++spins) { feed(mp_ok()); __builtin_amdgcn_s_sleep(4); }
    cn.wait<4>(C_F3, 4);
    wo_chunk(0); wo_chunk(4);
    cn.wait<8>(C_MP + 2, 4);
    STAMP(27);
    wo_chunk(1);
    if (!wo23) { wo_chunk(2); wo_chunk(3); }
    u32x4 qv[3];
#pragma unroll
    for (int i = 0; i < 3; i++) qv[i] = __builtin_amdgcn_raw_buffer_load_b128(rq, vq[i], 160 * hg, 0);
    STAMP(29);
    WAITV(3);
    cn.signal<C_WO>();
    STAMP(9);
    if (j == 1) { flag_poll<4>(rws, fpoll, WS_F3 + (hg * 32 + rt) * 256, tag); cn.set(C_P3, cn.one); }
    else cn.wait<5>(C_P3, 1);
    STAMP(28);
#pragma unroll
    for (int i = 0; i < 3; i++) if (j + 4 * i < 10) dma16c(rws, zi, i * 131072, L_ZB + 4096 * i + 1024 * j);
    WAITV(0);
    if (cn.arrive<C_AUX>() < 7) cn.wait<3>(C_AUX, 8);
    STAMP(15);
    cn.wait<4>(C_OPW + j, 1);
    STAMP(10);
    const int sl = (tp * 16 + (lane >> 2)) * 4;
#pragma unroll
    for (int i = 0; i < 3; i++) {
      if (j + 4 * i < 10) {
        const u32x2 y0 = trd(zl[i]), y1 = trd(zl[i] + 512);
        const u32x4 z = {(unsigned)__builtin_amdgcn_ds_bpermute(sl, y0[0]), (unsigned)__builtin_amdgcn_ds_bpermute(sl, y0[1]),
                         (unsigned)__builtin_amdgcn_ds_bpermute(sl, y1[0]), (unsigned)__builtin_amdgcn_ds_bpermute(sl, y1[1])};
        u32x4 r;
#pragma unroll
        for (int e = 0; e < 4; e++) r[e] = pk2(vmul(lo2f(qv[i][e]), lo2f(z[e])), vmul(hi2f(qv[i][e]), hi2f(z[e])));
        st128<0>(r, rout, 2 * vq[i] - (vq[i] & 255), 160 * hg);
      }
    }
    if (b == 0 && j == 3) {
      cn.wait<4>(C_F4, 1);
      if (lane == 0) *(u64*)(P.ws + WS_EPOCH) = ((u64)tag.hi << 16) | (tag.lo >> 16);
    }
  } else {
    const int rs = wave & 1, kh = wave >> 1;
    unsigned xa[4];
#pragma unroll
    for (int i = 0; i < 4; i++) xa[i] = L_X + (4 * rs + (col >> 3)) * 1024 + (col & 7) * 128 + 16 * ((2 * i + h) ^ ((col >> 1) & 7));
    const unsigned wa = L_WQ + (8 * h + 2 * tq) * 160 + 80 * kh + wpc;
    const int vrow = (col < 18) ? h * 20 + col : (col == 20 || col == 21) ? h * 20 + col - 2 : (col == 18 || col == 22) ? 40 : 41;
    const unsigned ok = lane * 16, ok2 = K2OFF + (h ? 256 : col * 8), ov = VOFF + vrow * 16;
    unsigned pk = L_SA + ok, pk2_ = L_SA + ok2, pv = L_SA + ov;
    const f32x16 zero = {0};
    f32x16 o[2], s[3];
    Rec f, f2;
    int idx = 0xdddd;
    o[0] = zero; o[1] = zero; s[2] = zero; f2.v[3] = u32x4{0, 0, 0, 0};
    unsigned wm = L_WO + (20 * (g & 1) + 8 * (g >> 1) + tq) * 160 + tp * 8;
    unsigned wr = L_WO + (80 * (g & 1) + 20 * (tq >> 1) + 16 + 2 * (g >> 1) + (tq & 1)) * 160 + tp * 8;
    unsigned ca = L_MISC + 4 * (C_KVS + 2), ma = L_MISC + 4 * (lane ? 48 + lane : C_MP);
    asm volatile("" : "+v"(o[0]), "+v"(o[1]), "+v"(s[2]), "+v"(f2.v[3]), "+v"(idx), "+v"(pk), "+v"(pk2_), "+v"(pv), "+v"(ca), "+v"(ma), "+v"(wm), "+v"(wr));
    f32x16 acc0 = zero, acc1 = zero;
    bf16x8 xf[2][4], a0[2][4], a1[2][4];
    int hc = 2 * cc0;
    asm volatile("s_waitcnt vmcnt(0)" : "+v"(cjunk), "+v"(pw[0]), "+v"(pw[1]), "+v"(pw[2]) :: "memory");
    if (prod) cn.wait<2>(C_LA, 2);
    u32x2 lv = {0, 0};
#pragma unroll
    for (int c = -1; c < 10; c++) {
      const int z = c & 1, n = z ^ 1;
      const unsigned w0 = wa + ((c + 1) % 5) * SLOT, xo = hc * 8192;
      if (c < 9) {
        if (__builtin_expect(rfl(lv[n]) < (unsigned)((c + 1) >> 1) + 1, 0)) cn.wait<1>(C_LAND + n, ((c + 1) >> 1) + 1);
        asm volatile("" ::: "memory");
        if (c == -1) STAMP(1);
        if (c == 8) STAMP(15);
        if (c == 2) STAMP(7);
        if (c == 5) STAMP(8);
      }
#pragma unroll
      for (int k = 0; k < 4; k++) {
        SB();
        if (c >= 0) acc0 = MF32(a0[z][k], xf[z][k], acc0);
        SB();
        if (c < 9) { xf[n][k] = *lp<bf16x8>(xa[k] + xo); a0[n][k] = cat2(trd(w0 + k * 2560), trd(w0 + k * 2560 + 160)); }
        SB();
        if (c >= 0) acc1 = MF32(a1[z][k], xf[z][k], acc1);
        SB();
        if (c < 8 && k == 2) lv = *(volatile LAS u32x2*)(size_t)(L_MISC + 4 * C_LAND);
        if (c == 9 && k == 0) lv[0] = *(volatile LAS unsigned*)(size_t)(L_MISC + 4 * C_KVS);
        if (c < 9) a1[n][k] = cat2(trd(w0 + k * 2560 + 40), trd(w0 + k * 2560 + 200));
      }
      if (c < 4 || c == 5 || c == 8) cn.set(C_FREE + wave, c + 2);
      hc = hc == 9 ? 0 : hc + 1;
      SB();
    }
    STAMP(2);
    if (rfl(lv[0]) < 1) cn.wait<0>(C_KVS, 1);
    asm volatile("" ::: "memory");
    STAMP(4);
    const unsigned kmw = *lp<unsigned>(L_MISC + 4 * W_KM);
    f.k2[0] = *lp<u32x2>(pk2_); f.k1[0] = *lp<u32x4>(pk);
    f.v[0] = *lp<u32x4>(pv); SB(); f.v[1] = *lp<u32x4>(pv + VFR); f.k1[1] = *lp<u32x4>(pk + 1024); f.k2[1] = *lp<u32x2>(pk2_ + K2S); f.v[2] = *lp<u32x4>(pv + 2 * VFR); SB(); f.v[3] = *lp<u32x4>(pv + 3 * VFR);
    SB();
    bf16x8 q1[2]; s16x4 q2[2];
#pragma unroll
    for (int t = 0; t < 2; t++) {
      const f32x16& a = t ? acc1 : acc0;
      u32x4 w = {pk2(a[0], a[1]), pk2(a[2], a[3]), pk2(a[4], a[5]), pk2(a[6], a[7])};
      u32x2 w2 = {pk2(a[8], a[9]), pk2(a[10], a[11])};
      float n2 = xhalf_sum(dot2(w[0], w[0], dot2(w[1], w[1], 0.f)) + dot2(w[2], w[2], dot2(w[3], w[3], 0.f)));
      n2 = dot2(w2[0], w2[0], dot2(w2[1], w2[1], n2));
      if (h) w2 = u32x2{pk2(__builtin_amdgcn_sqrtf(n2) * __uint_as_float(kmw) * -1.01f, 0.f), 0u};
      q1[t] = asb(w); q2[t] = as4(w2);
    }
    s[0] = MF32(asb(f.k1[0]), q1[0], MF8(as4(f.k2[0]), q2[0], zero));
    STAMP(5);
    {
      int dl = (int)L_SB - (int)L_SA;
      unsigned cv_ = 0, cs_ = 0;
      auto N = []() {};
      int it = 0;
#define REC_(r, c, n, h3, h6) lstep(c, n, s, o, q1, q2, idx, pk + ((r) + 1) * REC, pk2_ + ((r) + 1) * REC, pv + ((r) + 1) * REC, h3, h6)
      __builtin_amdgcn_s_waitcnt(lgenc(5));
#pragma unroll 1
      do {
        STAMPI(16 + it);
        REC_(0, f, f2, N, [&]() { asm volatile("" : "+v"(s[0])); if (__builtin_expect(it == 0, 0)) cn.wait<1>(C_KVS + 1, 2); });
        REC_(1, f2, f, N, N); REC_(2, f, f2, N, N); REC_(3, f2, f, N, N);
        REC_(4, f, f2, [&]() { cv_ = *(volatile LAS unsigned*)(size_t)ca; }, N);
        REC_(5, f2, f, [&]() { cs_ = rfl(cv_); }, N);
        REC_(6, f, f2, [&]() { asm volatile("" : "+v"(s[0])); if (__builtin_expect(cs_ < 2, 0)) cn.wait<1>(C_KVS + 2 + it, 2); asm volatile("" ::: "memory"); },
             [&]() { asm volatile("ds_add_u32 %0, %1" :: "v"(ma), "v"(cn.one) : "memory"); });
        lstep(f2, f, s, o, q1, q2, idx, pk + dl, pk2_ + dl, pv + dl, N, N);
        pk += dl; pk2_ += dl; pv += dl; dl = -dl;
        ca += 4; ma += 4;
      } while (++it < 4);
      o[1] = PV(asb(f2.v[3]), sub8<8>(s[2]), o[1]);
    }
    cn.set(C_OPW + wave, cn.one);
    STAMP(6);
    if (__builtin_expect(!__all(o[0][10] >= 9.0e-13f && o[1][10] >= 9.0e-13f), 0)) {
      float mx[2] = {-3.0e38f, -3.0e38f};
#pragma unroll
      for (int t = 0; t < 2; t++) { if (h) q2[t] = s16x4{0, 0, 0, 0}; o[t] = zero; }
#pragma unroll 1
      for (int pass = 0; pass < 2; pass++) {
#pragma unroll 1
        for (int r = 0; r < 64; r++) {
          const unsigned so = WS_IMG + (r >> 1) * REC;
          const u32x4 k1 = __builtin_amdgcn_raw_buffer_load_b128(rws, ok, so + (r & 1) * 1024, SC1);
          const u32x2 k2 = __builtin_amdgcn_raw_buffer_load_b64(rws, ok2, so + (r & 1) * K2S, SC1);
          const u32x4 va = __builtin_amdgcn_raw_buffer_load_b128(rws, ov, so + (r & 1) * 2 * VFR, SC1);
          const u32x4 vb = __builtin_amdgcn_raw_buffer_load_b128(rws, ov, so + (r & 1) * 2 * VFR + VFR, SC1);
#pragma unroll
          for (int t = 0; t < 2; t++) {
            f32x16 sv = MF32(asb(k1), q1[t], MF8(as4(k2), q2[t], zero));
            if (pass == 0) {
#pragma unroll
              for (int i = 0; i < 16; i++) mx[t] = fmaxf(mx[t], sv[i]);
            } else {
              exps<0, 16>(sv);
              o[t] = PV(asb(va), sub8<0>(sv), o[t]);
              o[t] = PV(asb(vb), sub8<8>(sv), o[t]);
            }
          }
        }
        if (pass == 0) {
#pragma unroll
          for (int t = 0; t < 2; t++) {
            const float m = -fmaxf(mx[t], __shfl_xor(mx[t], 32));
            const float m0 = hi2f(__float_as_uint(m)), m1 = hi2f(__float_as_uint(m - m0)), m2 = m - m0 - m1;
            if (h) q2[t] = as4(u32x2{pk2(m0, m1), pk2(m2, 0.f)});
          }
        }
      }
    }
    {
      const unsigned ox = WS_OX + ((rt * 8 + hg) * 2 + rs) * 5120;
      u32x2 e;
      auto pkv = [](float a, float b) -> unsigned { return umin(pk2(a, b), 0xfffffffeu); };
#pragma unroll
      for (int t = 0; t < 2; t++) {
        const float inv = __builtin_amdgcn_rcpf(o[t][10]);
        st128<SC1>(u32x4{pkv(o[t][0] * inv, o[t][1] * inv), pkv(o[t][2] * inv, o[t][3] * inv), pkv(o[t][4] * inv, o[t][5] * inv), pkv(o[t][6] * inv, o[t][7] * inv)},
                                               rws, ok, ox + (2 * kh + t) * 1024);
        e[t] = pkv(o[t][8] * inv, o[t][9] * inv);
      }
      __builtin_amdgcn_raw_buffer_store_b64(e, rws, ok, ox + 4096 + 8 * kh, SC1);
    }
    STAMP(10);
    constexpr int NPRE = NPRE_;
    unsigned wm2 = wm + 51200, wr2 = wr + 51200;
    asm volatile("" : "+v"(wm2), "+v"(wr2));
    auto wfrag = [&](int i, int m) {
      const unsigned a = i < 8 ? wm + i * 6400 : i < 16 ? wm2 + (i - 8) * 6400 : i < 18 ? wr + (i - 16) * 25600 : wr2 + (i - 18) * 25600;
      const u32x2 x = trd(a + m * 32), y = trd(a + m * 32 + (i < 16 ? 640 : 6400));
      return u32x4{x[0], x[1], y[0], y[1]};
    };
    unsigned fr[3], fo[3];
    const unsigned ra = L_RED + wave * 2816;
#pragma unroll
    for (int z = 0; z < 3; z++) {
      const unsigned u = z * 64 + lane, row = (u * 6554u) >> 16, cc = u - row * 10;
      fr[z] = ra + row * 176 + cc * 16; fo[z] = u < 160 ? (64 * rt + 16 * wave + row) * 2560 + 16 * cc : OOB;
      asm volatile("" : "+v"(fr[z]), "+v"(fo[z]));
    }
    unsigned fw = ra + c16 * 176 + g * 8;
    asm volatile("" : "+v"(fw));
    const unsigned ob = ((g >> 1) * 32 + (wave & 1) * 16 + c16) * 16, os = WS_OX + (rt * 16 + (wave >> 1)) * 5120;
    const unsigned obm = ob + (g & 1) * 1024, obr = ob + (g & 1) * 10240 + 4096;
    const unsigned po = (col >> 1) * 5120 + 4096 + (col & 1) * 8 + h * 1012;
    cn.wait<1>(C_WO, 4);
    f32x4 acc[5];
#pragma nounroll
    for (unsigned tries = 0; tries < SPIN_GLB; ++tries) {
      u32x4 wf[NPRE][5];
#pragma unroll
      for (int i = 0; i < NPRE; i++)
#pragma unroll
        for (int m = 0; m < 5; m++) wf[i][m] = wfrag(i, m);
      SB();
      STAMP(3);
      if (wave == 0) {
        cn.wait4<0>(C_OPW, 1);
#pragma nounroll
        for (unsigned spins = 0; spins < SPIN_GLB; ++spins) {
          asm volatile("" ::: "memory");
          if (__all(__builtin_amdgcn_raw_buffer_load_b32(rws, po, WS_OX + rt * 81920, SC1) != ~0u)) break;
          __builtin_amdgcn_s_sleep(PSL);
        }
        cn.set(C_F4, cn.one);
      } else cn.wait<0>(C_F4, 1);
      STAMP(11);
      u32x4 of[20];
      auto oload = [&](int i) {
        of[i] = i < 16 ? __builtin_amdgcn_raw_buffer_load_b128(rws, obm, os + (i >> 1) * 10240 + (i & 1) * 2048, SC1)
                       : __builtin_amdgcn_raw_buffer_load_b128(rws, obr, os + (i - 16) * 20480, SC1);
      };
#pragma unroll
      for (int i = 0; i < OWIN; i++) { oload(i); SB(); }
      unsigned bad = 0;
#pragma unroll
      for (int i = 0; i < 20; i++) {
#pragma unroll
        for (int m = 0; m < 5; m++) {
          SB();
          if (i == 0) asm volatile("v_mfma_f32_16x16x32_bf16 %0, %1, %2, 0" : "=&v"(acc[m]) : "v"(wf[0][m]), "v"(of[0]));
          else MF16A(acc[m], wf[i % NPRE][m], of[i]);
          SB();
          if (m == 0 && i + OWIN < 20) oload(i + OWIN);
          if (i + NPRE < 20) wf[i % NPRE][m] = wfrag(i + NPRE, m);
          if (m == 3) bad = umax(umax(bad, of[i][0]), of[i][1]);
          if (m == 4) bad = umax(umax(bad, of[i][2]), of[i][3]);
        }
      }
      asm volatile("s_nop 7" : "+v"(acc[0]), "+v"(acc[1]), "+v"(acc[2]), "+v"(acc[3]), "+v"(acc[4]));
      if (__all(bad != ~0u)) break;
    }
    STAMP(12);
#pragma unroll
    for (int m = 0; m < 5; m++) *lp<u32x2>(fw + m * 32) = u32x2{pk2(acc[m][0], acc[m][1]), pk2(acc[m][2], acc[m][3])};
    asm volatile("" ::: "memory");
    u32x4 fy[3];
#pragma unroll
    for (int z = 0; z < 3; z++) fy[z] = *lp<u32x4>(fr[z]);
#pragma unroll
    for (int z = 0; z < 3; z++) st128<0>(fy[z], rout, fo[z], 1280 + 160 * hg);
    STAMP(13);
    STAMP_OUT();
    __builtin_amdgcn_endpgm();
  }
  {
    STAMP(13);
    STAMP_OUT();
  }
}

extern "C" void kernel_launch(void* const* d_in, const int* in_sizes, int n_in,
                              void* d_out, int out_size, void* d_ws, size_t ws_size,
                              hipStream_t stream) {
  Params P;
  P.Aq = (const u16*)d_in[0]; P.Ak = (const u16*)d_in[1]; P.Av = (const u16*)d_in[2];
  P.sh = (const u16*)d_in[3]; P.hk = (const u16*)d_in[4]; P.Dv = (const u16*)d_in[5];
  P.x = (const u16*)d_in[6]; P.wq = (const u16*)d_in[7]; P.wk = (const u16*)d_in[8];
  P.wv = (const u16*)d_in[9]; P.wo = (const u16*)d_in[10];
  P.out = (u16*)d_out; P.ws = (char*)d_ws;
  (void)hipFuncSetAttribute((const void*)fused_kernel, hipFuncAttributeMaxDynamicSharedMemorySize, LDS_BYTES);
  fused_kernel<<<256, 512, LDS_BYTES, stream>>>(P);
}
```
